```python
import torch

def _softmax(x, axis=-1):
    return torch.softmax(x, dim=axis)
def _gelu(x):
    return torch.nn.functional.gelu(x, approximate="tanh")
def _silu(x):
    return torch.nn.functional.silu(x)
def _sigmoid(x):
    return torch.sigmoid(x)
def _layernorm(x, axis=-1, eps=1e-5):
    m = x.mean(dim=axis, keepdim=True)
    v = x.var(dim=axis, keepdim=True, unbiased=False)
    return (x - m) / torch.sqrt(v + eps)
def _scatter_add(tgt, idx, src, axis=0):
    return tgt.clone().index_add_(axis, idx, src)
def _segment_sum(src, seg, n):
    out = torch.zeros((n,) + src.shape[1:], dtype=src.dtype)
    return out.index_add_(0, seg, src)
def _pad(x, pw):
    flat = tuple(v for pair in reversed(pw) for v in pair)
    return torch.nn.functional.pad(x, flat)

kernel_name = 'gather_scatter__mpnn_edge_mlp__graph'

def setup_inputs():

    _w = (lambda a: (lambda t: t.to(torch.bfloat16) if t.is_floating_point() else t)(torch.as_tensor(a)))
    N, deg, D, De = 10000, 16, 128, max(4, 1)
    E = N * deg
    return {
        "x": _w((lambda *s: torch.randn(*s, dtype=torch.bfloat16) * 0.05)(N, D)),
        "e_attr": _w((lambda *s: torch.randn(*s, dtype=torch.bfloat16) * 0.4)(E, De)),
        "src": _w((lambda lo, hi, *s: torch.randint(lo, hi, s))(0, N, E).to(torch.int64)),
        "dst": _w((lambda lo, hi, *s: torch.randint(lo, hi, s))(0, N, E).to(torch.int64)),
        "w_msg": _w((lambda *s: torch.randn(*s, dtype=torch.bfloat16) * 0.4)(2 * D + De, D)),
        "w_upd": _w((lambda *s: torch.randn(*s, dtype=torch.bfloat16) * 0.05)(2 * D, D)),
    }

def reference(x, e_attr, src, dst, w_msg, w_upd):
    N, D = x.shape
    m_in = torch.concatenate([x[src], x[dst], e_attr], axis=-1)
    msg = _silu(m_in @ w_msg)
    agg = _segment_sum(msg, dst, N)
    return _silu(torch.concatenate([x, agg], axis=-1) @ w_upd)
```

```cpp
#include <hip/hip_runtime.h>
#include <hip/hip_bf16.h>
#include <stdint.h>

static __device__ __forceinline__ float bf2f(uint16_t v) { return __uint_as_float((uint32_t)v << 16); }
static __device__ __forceinline__ float silu(float v) { return v / (1.0f + __expf(-v)); }

__global__ void k_pq(const uint16_t* x, const uint16_t* wm, float* P, float* Q, float* agg, int N) {
    int n = blockIdx.x, c = threadIdx.x;
    float p = 0.f, q = 0.f;
    for (int k = 0; k < 128; ++k) {
        float xv = bf2f(x[n * 128 + k]);
        p += xv * bf2f(wm[k * 128 + c]);
        q += xv * bf2f(wm[(128 + k) * 128 + c]);
    }
    P[n * 128 + c] = p; Q[n * 128 + c] = q; agg[n * 128 + c] = 0.f;
}
__global__ void k_edge(const uint16_t* ea, const int* src, const int* dst, const uint16_t* wm,
                       const float* P, const float* Q, float* agg, int E) {
    int e = blockIdx.x, c = threadIdx.x;
    int s = src[e], d = dst[e];
    float v = P[s * 128 + c] + Q[d * 128 + c];
    for (int k = 0; k < 4; ++k) v += bf2f(ea[e * 4 + k]) * bf2f(wm[(256 + k) * 128 + c]);
    atomicAdd(&agg[d * 128 + c], silu(v));
}
__global__ void k_upd(const uint16_t* x, const float* agg, const uint16_t* wu, __hip_bfloat16* out, int N) {
    int n = blockIdx.x, c = threadIdx.x;
    float v = 0.f;
    for (int k = 0; k < 128; ++k) {
        v += bf2f(x[n * 128 + k]) * bf2f(wu[k * 128 + c]);
        v += __bfloat162float(__float2bfloat16(agg[n * 128 + k])) * bf2f(wu[(128 + k) * 128 + c]);
    }
    out[n * 128 + c] = __float2bfloat16(silu(v));
}

extern "C" void kernel_launch(void* const* d_in, const int* in_sizes, int n_in,
                              void* d_out, int out_size, void* d_ws, size_t ws_size,
                              hipStream_t stream) {
    const uint16_t* x = (const uint16_t*)d_in[0];
    const uint16_t* ea = (const uint16_t*)d_in[1];
    const int* src = (const int*)d_in[2];
    const int* dst = (const int*)d_in[3];
    const uint16_t* wm = (const uint16_t*)d_in[4];
    const uint16_t* wu = (const uint16_t*)d_in[5];
    int N = in_sizes[0] / 128, E = in_sizes[2];
    float* P = (float*)d_ws; float* Q = P + (size_t)N * 128; float* agg = Q + (size_t)N * 128;
    k_pq<<<N, 128, 0, stream>>>(x, wm, P, Q, agg, N);
    k_edge<<<E, 128, 0, stream>>>(ea, src, dst, wm, P, Q, agg, E);
    k_upd<<<N, 128, 0, stream>>>(x, agg, wu, (__hip_bfloat16*)d_out, N);
}
```

```cpp
#include <hip/hip_runtime.h>
#include <stdint.h>

typedef unsigned u32;

static __device__ __forceinline__ float bf2f(uint16_t v) { return __uint_as_float((u32)v << 16); }
static __device__ __forceinline__ float silu(float v) { return v / (1.0f + __expf(-v)); }
static __device__ __forceinline__ uint16_t f2bf(float v) { return __builtin_bit_cast(uint16_t, (__bf16)v); }

static __device__ __forceinline__ void slow_rows(char* smem, const uint16_t* x, const uint16_t* ea, const int* src, const int* dst,
                                                 const uint16_t* wm, const uint16_t* wu, uint16_t* out, int N, int E, int first) {
    const int tid = threadIdx.x;
    float* agg = (float*)smem;
    int* list = (int*)(smem + 20480);
    int* cnt = (int*)(smem + 22528);
    __syncthreads();
    for (int i = tid; i < 40 * 128; i += 512) agg[i] = 0.f;
    for (int e0 = 0; e0 < E; e0 += 512) {
        if (tid == 0) *cnt = 0;
        __syncthreads();
        const int e = e0 + tid;
        if (e < E) {
            const int r = dst[e] - first;
            if (r >= 0 && r < 40 * 256 && (r & 255) == 0) list[atomicAdd(cnt, 1)] = e;
        }
        __syncthreads();
        const int n = *cnt, c = tid & 127;
        for (int i = tid >> 7; i < n; i += 4) {
            const int ee = list[i], s = src[ee], d = dst[ee];
            float v = 0.f;
#pragma unroll 4
            for (int k = 0; k < 128; ++k)
                v += bf2f(x[(size_t)s * 128 + k]) * bf2f(wm[k * 128 + c]) + bf2f(x[(size_t)d * 128 + k]) * bf2f(wm[(128 + k) * 128 + c]);
            for (int k = 0; k < 4; ++k) v += bf2f(ea[(size_t)ee * 4 + k]) * bf2f(wm[(256 + k) * 128 + c]);
            atomicAdd(&agg[((d - first) >> 8) * 128 + c], bf2f(f2bf(silu(v))));
        }
        __syncthreads();
    }
    for (int i = tid; i < 40 * 128; i += 512) {
        const int j = i >> 7, c = i & 127, node = first + 256 * j;
        if (node < N) {
            float v = 0.f;
#pragma unroll 4
            for (int k = 0; k < 128; ++k)
                v += bf2f(x[(size_t)node * 128 + k]) * bf2f(wu[k * 128 + c]) + bf2f(f2bf(agg[j * 128 + k])) * bf2f(wu[(128 + k) * 128 + c]);
            out[(size_t)node * 128 + c] = f2bf(silu(v));
        }
    }
}

__global__ void __launch_bounds__(512, 2)
mpnn_kernel(const uint16_t* x, u32 nx, u32 f0, const uint16_t* ea, u32 ne, u32 f1, const int* src, u32 ns, u32 f2, const int* dst, u32 nd, u32 f3,
            const uint16_t* wm, u32 nm, u32 f4, const uint16_t* wu, u32 nu, u32 f5, uint16_t* out, u32 no, u32 f6, void* ws, u32 nw, u32 f7,
            void* ptab, u32 np, u32 f8, int E, int CH) {
#if defined(__HIP_DEVICE_COMPILE__)
    __shared__ __attribute__((aligned(16))) char smem[163840];
    u32 slow;
    asm volatile(
        "s_load_dwordx16 s[36:51], %1, 0x0\n"
        "s_load_dwordx16 s[52:67], %1, 0x40\n"
        "s_load_dwordx4 s[68:71], %1, 0x80\n"
        "s_load_dwordx2 s[72:73], %1, 0x90\n"
        "s_getpc_b64 s[90:91]\n"
        "L_pc%=:\n"
        "v_lshrrev_b32 v2, 6, v0\n"
        "v_and_b32 v1, 63, v0\n"
        "v_readfirstlane_b32 s8, v2\n"
        "s_mov_b32 %0, 0\n"
        "s_and_b32 s16, %2, 7\n"
        "s_lshl_b32 s16, s16, 3\n"
        "s_bfe_u32 s17, %2, 0x30003\n"
        "s_or_b32 s16, s16, s17\n"
        "s_and_b32 s11, %2, 0xc0\n"
        "s_or_b32 s9, s11, s16\n"
        "v_lshrrev_b32 v6, 4, v1\n"
        "v_and_b32 v7, 15, v1\n"
        "s_lshr_b32 s14, s8, 2\n"
        "s_and_b32 s13, s8, 3\n"
        "s_and_b32 s15, s8, 1\n"
        "s_lshl_b32 s12, s8, 12\n"
        "s_add_u32 s12, s12, 65536\n"
        "v_lshlrev_b32 v4, 2, v6\n"
        "v_xor_b32 v3, v7, v4\n"
        "v_lshlrev_b32 v2, 8, v6\n"
        "v_lshl_or_b32 v8, v3, 4, v2\n"
        "s_lshl_b32 s17, s8, 2\n"
        "s_lshr_b32 s17, 0x11234454, s17\n"
        "s_and_b32 s17, s17, 7\n"
        "s_lshl_b32 s33, s17, 2\n"
        "v_lshlrev_b32 v5, 7, v1\n"
        "s_lshl_b32 s96, s13, 13\n"
        "s_cmp_ge_u32 s8, 4\n"
        "s_cbranch_scc1 L_brole%=\n"
        "s_setprio 3\n"
        "v_xor_b32 v3, v7, v6\n"
        "s_lshl_b32 s17, s9, 8\n"
        "s_lshl_b32 s18, s8, 18\n"
        "s_add_u32 s17, s17, s18\n"
        "v_lshl_add_u32 v3, v3, 4, s17\n"
        "v_lshl_add_u32 v126, v6, 16, v3\n"
        "v_add_u32 v127, 0x100000, v126\n"
        "v_add_u32 v128, 0x200000, v126\n"
        "s_mul_i32 s18, s8, 1088\n"
        "s_add_u32 s18, s18, 32768\n"
        "s_mov_b32 m0, s18\n"
        "s_lshr_b32 s16, %2, 3\n"
        "s_lshl_b32 s16, s16, 11\n"
        "s_cmp_ge_u32 s8, 2\n"
        "s_cselect_b32 s16, 0xfc00, s16\n"
        "s_waitcnt lgkmcnt(0)\n"
        "buffer_load_dwordx4 v126, s[36:39], 0 offen lds\n"
        "s_add_u32 m0, s18, 4352\n"
        "s_cmp_ge_u32 s8, 2\n"
        "buffer_load_dwordx4 v127, s[36:39], 0 offen lds\n"
        "s_cbranch_scc1 L_gx%=\n"
        "s_add_u32 m0, s18, 8704\n"
        "s_nop 0\n"
        "buffer_load_dwordx4 v128, s[36:39], 0 offen lds\n"
        "L_gx%=:\n"
        "s_mov_b32 m0, s33\n"
        "s_nop 0\n"
        "s_movrels_b64 s[92:93], s[36:37]\n"
        "s_movrels_b64 s[94:95], s[38:39]\n"
        "s_add_u32 s18, s96, 131072\n"
        "s_mov_b32 m0, s18\n"
        "s_mov_b64 exec, 0xffff\n"
        "s_cmp_lg_u32 s8, 3\n"
        "s_cbranch_scc1 L_ncp%=\n"
        "s_mov_b64 s[92:93], s[90:91]\n"
        "s_mov_b32 s94, L_end%=-L_pc%=\n"
        "s_lshr_b32 s16, %2, 3\n"
        "s_mul_i32 s16, s16, 512\n"
        "s_mov_b32 exec_lo, 0x1f\n"
        "L_ncp%=:\n"
        "buffer_load_dword v125, v5, s[92:95], s16 offen\n"
        "s_mov_b64 exec, -1\n"
        "s_lshl_b32 s17, s8, 8\n"
        "s_add_u32 s17, s17, 65536\n"
        "v_lshlrev_b32 v2, 4, v1\n"
        "s_mov_b64 exec, 0xffff\n"
        "buffer_load_dwordx4 v[104:107], v2, s[52:55], s17 offen\n"
        "s_mov_b64 exec, -1\n"
        "buffer_load_dword v124, off, s[64:67], 0 sc1\n"
        "v_mov_b32 v102, 0\n"
        "v_and_b32 v9, 31, v1\n"
        "v_lshrrev_b32 v10, 5, v1\n"
        "v_bfe_u32 v11, v1, 2, 2\n"
        "v_and_b32 v12, 3, v1\n"
        "v_bfe_u32 v13, v1, 4, 1\n"
        "v_xor_b32 v2, s13, v11\n"
        "v_and_b32 v3, 1, v12\n"
        "v_lshl_or_b32 v3, v3, 1, v13\n"
        "v_lshl_or_b32 v2, v2, 2, v3\n"
        "v_lshlrev_b32 v3, 2, v12\n"
        "v_and_b32 v3, 8, v3\n"
        "v_lshl_or_b32 v2, v2, 4, v3\n"
        "v_lshl_or_b32 v2, v11, 8, v2\n"
        "v_add_u32 v14, 131072, v2\n"
        "v_lshl_add_u32 v15, v10, 11, v14\n"
        "v_lshrrev_b32 v2, 2, v9\n"
        "v_mul_u32_u24 v2, 1088, v2\n"
        "v_xor_b32 v3, v12, v10\n"
        "v_lshl_add_u32 v2, v3, 4, v2\n"
        "v_lshl_add_u32 v2, v12, 8, v2\n"
        "s_mul_i32 s19, s14, 8704\n"
        "s_add_u32 s19, s19, 32768\n"
        "v_add_u32 v16, s19, v2\n"
        "v_xor_b32 v17, 32, v16\n"
        "s_mov_b32 s20, 0xbfb8aa3b\n"
        "s_mov_b32 s21, 0xbfb8aa3b\n"
        "s_mov_b32 s26, 0x000fffff\n"
        "s_mov_b32 s27, 0x000fffff\n"
        "s_mul_i32 s19, s14, 1310720\n"
        "s_lshl_b32 s18, s9, 8\n"
        "s_add_u32 s19, s19, s18\n"
        "s_lshl_b32 s18, s13, 6\n"
        "s_add_u32 s19, s19, s18\n"
        "v_lshrrev_b32 v3, 2, v1\n"
        "v_lshl_add_u32 v2, v12, 4, s19\n"
        "v_lshl_add_u32 v18, v3, 16, v2\n"
        "v_bfe_u32 v2, v1, 3, 2\n"
        "v_xor_b32 v2, v2, v12\n"
        "v_lshlrev_b32 v2, 4, v2\n"
        "v_lshl_or_b32 v20, v3, 6, v2\n"
        "v_bfe_u32 v2, v9, 1, 2\n"
        "v_lshlrev_b32 v3, 1, v10\n"
        "v_xor_b32 v2, v2, v3\n"
        "v_lshlrev_b32 v2, 4, v2\n"
        "v_lshl_or_b32 v19, v9, 6, v2\n"
        "s_mul_i32 s18, s8, 2560\n"
        "s_add_u32 s18, s18, 43776\n"
        "v_add_u32 v108, s18, v19\n"
        "v_xor_b32 v109, 16, v108\n"
        "v_xor_b32 v110, 32, v108\n"
        "v_xor_b32 v111, 48, v108\n"
        "v_add_u32 v103, s18, v20\n"
        "s_add_u32 s18, s96, 131072\n"
        "s_mov_b32 m0, s18\n"
        "s_add_u32 s17, s96, 0\n"
        "buffer_load_dwordx4 v8, s[52:55], s17 offen offset:0 lds\n"
        "buffer_load_dwordx4 v8, s[52:55], s17 offen offset:1024 lds\n"
        "buffer_load_dwordx4 v8, s[52:55], s17 offen offset:2048 lds\n"
        "buffer_load_dwordx4 v8, s[52:55], s17 offen offset:3072 lds\n"
        "s_add_u32 s18, s96, 135168\n"
        "s_mov_b32 m0, s18\n"
        "s_add_u32 s17, s96, 4096\n"
        "buffer_load_dwordx4 v8, s[52:55], s17 offen offset:0 lds\n"
        "buffer_load_dwordx4 v8, s[52:55], s17 offen offset:1024 lds\n"
        "buffer_load_dwordx4 v8, s[52:55], s17 offen offset:2048 lds\n"
        "buffer_load_dwordx4 v8, s[52:55], s17 offen offset:3072 lds\n"
        "s_barrier\n"
        "s_branch L_grest%=\n"
        "L_brole%=:\n"
        "s_setprio 2\n"
        "v_and_b32 v126, 0xff, v0\n"
        "v_lshlrev_b32 v127, 4, v126\n"
        "v_lshlrev_b32 v128, 3, v126\n"
        "v_lshlrev_b32 v129, 5, v126\n"
        "s_mov_b32 m0, s33\n"
        "s_waitcnt lgkmcnt(0)\n"
        "s_mul_i32 s90, s16, s73\n"
        "s_lshl_b32 s90, s90, 2\n"
        "s_movrels_b64 s[92:93], s[36:37]\n"
        "s_movrels_b64 s[94:95], s[38:39]\n"
        "s_mul_i32 s17, s11, 40\n"
        "s_add_u32 s17, s17, s90\n"
        "s_lshl_b32 s18, s17, 1\n"
        "s_cmp_ge_u32 s8, 6\n"
        "s_cselect_b32 s17, s18, s17\n"
        "s_add_u32 s18, s18, 2560\n"
        "s_cmp_ge_u32 s8, 7\n"
        "s_cselect_b32 s17, s18, s17\n"
        "s_mov_b64 exec, 0x1fffff\n"
        "buffer_load_dword v125, v5, s[92:95], s17 offen\n"
        "s_mov_b64 exec, -1\n"
        "v_mov_b64 v[120:121], 0\n"
        "v_mov_b64 v[122:123], 0\n"
        "v_add_u32 v4, 98304, v127\n"
        "ds_write_b128 v4, v[120:123] offset:0\n"
        "ds_write_b128 v4, v[120:123] offset:4096\n"
        "ds_write_b128 v4, v[120:123] offset:8192\n"
        "ds_write_b128 v4, v[120:123] offset:12288\n"
        "ds_write_b128 v4, v[120:123] offset:16384\n"
        "ds_write_b128 v4, v[120:123] offset:20480\n"
        "v_subrev_u32 v2, 64, v126\n"
        "v_mul_u32_u24 v3, 7168, v2\n"
        "v_cmp_gt_u32 vcc, 4, v2\n"
        "v_cmp_gt_u32 s[26:27], 64, v126\n"
        "s_movk_i32 s17, 76\n"
        "v_cndmask_b32 v3, 0, v3, vcc\n"
        "v_cmp_gt_u32 vcc, s17, v126\n"
        "v_cndmask_b32 v3, v3, 2, s[26:27]\n"
        "v_lshlrev_b32 v2, 2, v126\n"
        "s_mov_b64 exec, vcc\n"
        "ds_write_b32 v2, v3 offset:55040\n"
        "s_mov_b64 exec, -1\n"
        "ds_write_b128 v127, v[120:123] offset:0\n"
        "ds_write_b128 v127, v[120:123] offset:4096\n"
        "ds_write_b128 v127, v[120:123] offset:8192\n"
        "ds_write_b128 v127, v[120:123] offset:12288\n"
        "ds_write_b128 v127, v[120:123] offset:16384\n"
        "ds_write_b128 v127, v[120:123] offset:20480\n"
        "ds_write_b128 v127, v[120:123] offset:24576\n"
        "v_mov_b32 v120, 0x3fff\n"
        "v_mov_b32 v121, 0x3fff\n"
        "v_mov_b32 v122, 0x3fff\n"
        "v_mov_b32 v123, 0x3fff\n"
        "ds_write_b128 v127, v[120:123] offset:28672\n"
        "v_add_u32 v170, 98304, v127\n"
        "s_sub_u32 s17, s73, 2048\n"
        "s_lshr_b32 s17, s17, 1\n"
        "v_cmp_gt_u32 s[86:87], s17, v126\n"
        "v_mov_b32 v171, 1\n"
        "s_mov_b32 s33, 0x06050401\n"
        "s_mov_b32 s91, 98304\n"
        "s_mov_b32 s92, 0xfffea000\n"
        "v_lshrrev_b32 v7, 3, v126\n"
        "v_lshlrev_b32 v172, 2, v7\n"
        "v_add_u32 v173, 106496, v129\n"
        "s_mul_i32 s17, s9, 8320\n"
        "s_add_u32 s17, s17, 270336\n"
        "v_add_u32 v174, s17, v127\n"
        "v_and_b32 v176, 0x70, v127\n"
        "s_mul_i32 s17, s9, 16512\n"
        "s_add_u32 s17, s17, 2400256\n"
        "v_lshl_add_u32 v175, v7, 8, s17\n"
        "v_add_u32 v175, v175, v176\n"
        "v_add_u32 v172, 55040, v172\n"
        "v_lshrrev_b32 v103, 2, v176\n"
        "s_mov_b32 s24, 0x01010101\n"
        "s_mov_b32 s25, 0x01010101\n"
        "s_mov_b32 s98, 0x0f0f0f0f\n"
        "s_mov_b32 s99, 0x0f0f0f0f\n"
        "s_add_u32 s17, s90, 4096\n"
        "s_add_u32 s18, s90, 8192\n"
        "s_waitcnt lgkmcnt(0)\n"
        "s_barrier\n"
        "buffer_load_dwordx4 v[130:133], v127, s[48:51], s90 offen\n"
        "buffer_load_dwordx4 v[134:137], v127, s[48:51], s17 offen\n"
        "buffer_load_dwordx2 v[138:139], v128, s[48:51], s18 offen\n"
        "buffer_load_dwordx4 v[140:143], v127, s[44:47], s90 offen\n"
        "buffer_load_dwordx4 v[144:147], v127, s[44:47], s17 offen\n"
        "buffer_load_dwordx2 v[148:149], v128, s[44:47], s18 offen\n"
        "buffer_load_dword v124, off, s[64:67], 0 sc1\n"
        "s_lshl_b32 s90, s90, 1\n"
        "s_add_u32 s17, s90, 8192\n"
        "s_add_u32 s18, s90, 16384\n"
        "buffer_load_dwordx4 v[150:153], v129, s[40:43], s90 offen\n"
        "buffer_load_dwordx4 v[154:157], v129, s[40:43], s90 offen offset:16\n"
        "buffer_load_dwordx4 v[158:161], v129, s[40:43], s17 offen\n"
        "buffer_load_dwordx4 v[162:165], v129, s[40:43], s17 offen offset:16\n"
        "buffer_load_dwordx4 v[166:169], v127, s[40:43], s18 offen\n"
        "s_waitcnt vmcnt(9)\n"
        "v_xor_b32 v177, s11, v130\n"
        "v_cmp_gt_u32_sdwa s[26:27], 64, v177 src0_sel:DWORD src1_sel:BYTE_0\n"
        "v_lshlrev_b32_sdwa v177, 2, v177 dst_sel:DWORD dst_unused:UNUSED_PAD src0_sel:DWORD src1_sel:BYTE_0\n"
        "v_xor_b32 v178, s11, v131\n"
        "v_cmp_gt_u32_sdwa s[28:29], 64, v178 src0_sel:DWORD src1_sel:BYTE_0\n"
        "v_lshlrev_b32_sdwa v178, 2, v178 dst_sel:DWORD dst_unused:UNUSED_PAD src0_sel:DWORD src1_sel:BYTE_0\n"
        "v_xor_b32 v179, s11, v132\n"
        "v_cmp_gt_u32_sdwa s[30:31], 64, v179 src0_sel:DWORD src1_sel:BYTE_0\n"
        "v_lshlrev_b32_sdwa v179, 2, v179 dst_sel:DWORD dst_unused:UNUSED_PAD src0_sel:DWORD src1_sel:BYTE_0\n"
        "v_xor_b32 v180, s11, v133\n"
        "v_cmp_gt_u32_sdwa s[34:35], 64, v180 src0_sel:DWORD src1_sel:BYTE_0\n"
        "v_lshlrev_b32_sdwa v180, 2, v180 dst_sel:DWORD dst_unused:UNUSED_PAD src0_sel:DWORD src1_sel:BYTE_0\n"
        "v_xor_b32 v181, s11, v134\n"
        "v_cmp_gt_u32_sdwa s[74:75], 64, v181 src0_sel:DWORD src1_sel:BYTE_0\n"
        "v_lshlrev_b32_sdwa v181, 2, v181 dst_sel:DWORD dst_unused:UNUSED_PAD src0_sel:DWORD src1_sel:BYTE_0\n"
        "v_xor_b32 v182, s11, v135\n"
        "v_cmp_gt_u32_sdwa s[76:77], 64, v182 src0_sel:DWORD src1_sel:BYTE_0\n"
        "v_lshlrev_b32_sdwa v182, 2, v182 dst_sel:DWORD dst_unused:UNUSED_PAD src0_sel:DWORD src1_sel:BYTE_0\n"
        "v_xor_b32 v183, s11, v136\n"
        "v_cmp_gt_u32_sdwa s[78:79], 64, v183 src0_sel:DWORD src1_sel:BYTE_0\n"
        "v_lshlrev_b32_sdwa v183, 2, v183 dst_sel:DWORD dst_unused:UNUSED_PAD src0_sel:DWORD src1_sel:BYTE_0\n"
        "v_xor_b32 v184, s11, v137\n"
        "v_cmp_gt_u32_sdwa s[80:81], 64, v184 src0_sel:DWORD src1_sel:BYTE_0\n"
        "v_lshlrev_b32_sdwa v184, 2, v184 dst_sel:DWORD dst_unused:UNUSED_PAD src0_sel:DWORD src1_sel:BYTE_0\n"
        "v_xor_b32 v185, s11, v138\n"
        "v_cmp_gt_u32_sdwa s[82:83], 64, v185 src0_sel:DWORD src1_sel:BYTE_0\n"
        "v_lshlrev_b32_sdwa v185, 2, v185 dst_sel:DWORD dst_unused:UNUSED_PAD src0_sel:DWORD src1_sel:BYTE_0\n"
        "v_xor_b32 v186, s11, v139\n"
        "v_cmp_gt_u32_sdwa s[84:85], 64, v186 src0_sel:DWORD src1_sel:BYTE_0\n"
        "v_lshlrev_b32_sdwa v186, 2, v186 dst_sel:DWORD dst_unused:UNUSED_PAD src0_sel:DWORD src1_sel:BYTE_0\n"
        "s_and_b64 s[82:83], s[82:83], s[86:87]\n"
        "s_and_b64 s[84:85], s[84:85], s[86:87]\n"
        "s_mov_b64 exec, s[26:27]\n"
        "ds_add_rtn_u32 v187, v177, v171 offset:55040\n"
        "s_mov_b64 exec, s[28:29]\n"
        "ds_add_rtn_u32 v188, v178, v171 offset:55040\n"
        "s_mov_b64 exec, s[30:31]\n"
        "ds_add_rtn_u32 v189, v179, v171 offset:55040\n"
        "s_mov_b64 exec, s[34:35]\n"
        "ds_add_rtn_u32 v190, v180, v171 offset:55040\n"
        "s_mov_b64 exec, s[74:75]\n"
        "ds_add_rtn_u32 v191, v181, v171 offset:55040\n"
        "s_mov_b64 exec, s[76:77]\n"
        "ds_add_rtn_u32 v192, v182, v171 offset:55040\n"
        "s_mov_b64 exec, s[78:79]\n"
        "ds_add_rtn_u32 v193, v183, v171 offset:55040\n"
        "s_mov_b64 exec, s[80:81]\n"
        "ds_add_rtn_u32 v194, v184, v171 offset:55040\n"
        "s_mov_b64 exec, s[82:83]\n"
        "ds_add_rtn_u32 v195, v185, v171 offset:55040\n"
        "s_mov_b64 exec, s[84:85]\n"
        "ds_add_rtn_u32 v196, v186, v171 offset:55040\n"
        "s_mov_b64 exec, -1\n"
        "s_waitcnt vmcnt(5)\n"
        "v_readfirstlane_b32 s10, v124\n"
        "v_perm_b32 v140, v140, v130, s33\n"
        "v_lshl_add_u32 v177, v177, 5, s91\n"
        "v_perm_b32 v141, v141, v131, s33\n"
        "v_lshl_add_u32 v178, v178, 5, s91\n"
        "v_perm_b32 v142, v142, v132, s33\n"
        "v_lshl_add_u32 v179, v179, 5, s91\n"
        "v_perm_b32 v143, v143, v133, s33\n"
        "v_lshl_add_u32 v180, v180, 5, s91\n"
        "v_perm_b32 v144, v144, v134, s33\n"
        "v_lshl_add_u32 v181, v181, 5, s91\n"
        "v_perm_b32 v145, v145, v135, s33\n"
        "v_lshl_add_u32 v182, v182, 5, s91\n"
        "v_perm_b32 v146, v146, v136, s33\n"
        "v_lshl_add_u32 v183, v183, 5, s91\n"
        "v_perm_b32 v147, v147, v137, s33\n"
        "v_lshl_add_u32 v184, v184, 5, s91\n"
        "v_perm_b32 v148, v148, v138, s33\n"
        "v_lshl_add_u32 v185, v185, 5, s91\n"
        "v_perm_b32 v149, v149, v139, s33\n"
        "v_lshl_add_u32 v186, v186, 5, s91\n"
        "s_add_u32 s10, s10, 0x400001\n"
        "s_waitcnt lgkmcnt(0)\n"
        "v_and_b32 v187, 31, v187\n"
        "v_lshl_add_u32 v177, v187, 2, v177\n"
        "v_lshl_add_u32 v187, v177, 1, s92\n"
        "v_and_b32 v188, 31, v188\n"
        "v_lshl_add_u32 v178, v188, 2, v178\n"
        "v_lshl_add_u32 v188, v178, 1, s92\n"
        "v_and_b32 v189, 31, v189\n"
        "v_lshl_add_u32 v179, v189, 2, v179\n"
        "v_lshl_add_u32 v189, v179, 1, s92\n"
        "v_and_b32 v190, 31, v190\n"
        "v_lshl_add_u32 v180, v190, 2, v180\n"
        "v_lshl_add_u32 v190, v180, 1, s92\n"
        "v_and_b32 v191, 31, v191\n"
        "v_lshl_add_u32 v181, v191, 2, v181\n"
        "v_lshl_add_u32 v191, v181, 1, s92\n"
        "v_and_b32 v192, 31, v192\n"
        "v_lshl_add_u32 v182, v192, 2, v182\n"
        "v_lshl_add_u32 v192, v182, 1, s92\n"
        "v_and_b32 v193, 31, v193\n"
        "v_lshl_add_u32 v183, v193, 2, v183\n"
        "v_lshl_add_u32 v193, v183, 1, s92\n"
        "v_and_b32 v194, 31, v194\n"
        "v_lshl_add_u32 v184, v194, 2, v184\n"
        "v_lshl_add_u32 v194, v184, 1, s92\n"
        "v_and_b32 v195, 31, v195\n"
        "v_lshl_add_u32 v185, v195, 2, v185\n"
        "v_lshl_add_u32 v195, v185, 1, s92\n"
        "v_and_b32 v196, 31, v196\n"
        "v_lshl_add_u32 v186, v196, 2, v186\n"
        "v_lshl_add_u32 v196, v186, 1, s92\n"
        "s_waitcnt vmcnt(0)\n"
        "s_mov_b64 exec, s[26:27]\n"
        "ds_write_b32 v177, v140\n"
        "ds_write_b64 v187, v[150:151]\n"
        "s_mov_b64 exec, s[28:29]\n"
        "ds_write_b32 v178, v141\n"
        "ds_write_b64 v188, v[152:153]\n"
        "s_mov_b64 exec, s[30:31]\n"
        "ds_write_b32 v179, v142\n"
        "ds_write_b64 v189, v[154:155]\n"
        "s_mov_b64 exec, s[34:35]\n"
        "ds_write_b32 v180, v143\n"
        "ds_write_b64 v190, v[156:157]\n"
        "s_mov_b64 exec, s[74:75]\n"
        "ds_write_b32 v181, v144\n"
        "ds_write_b64 v191, v[158:159]\n"
        "s_mov_b64 exec, s[76:77]\n"
        "ds_write_b32 v182, v145\n"
        "ds_write_b64 v192, v[160:161]\n"
        "s_mov_b64 exec, s[78:79]\n"
        "ds_write_b32 v183, v146\n"
        "ds_write_b64 v193, v[162:163]\n"
        "s_mov_b64 exec, s[80:81]\n"
        "ds_write_b32 v184, v147\n"
        "ds_write_b64 v194, v[164:165]\n"
        "s_mov_b64 exec, s[82:83]\n"
        "ds_write_b32 v185, v148\n"
        "ds_write_b64 v195, v[166:167]\n"
        "s_mov_b64 exec, s[84:85]\n"
        "ds_write_b32 v186, v149\n"
        "ds_write_b64 v196, v[168:169]\n"
        "s_mov_b64 exec, -1\n"
        "s_and_b32 s93, s10, 0xffc000c0\n"
        "s_mul_i32 s94, s10, 0x9e3779b1\n"
        "s_and_b32 s94, s94, 0xffc000c0\n"
        "v_mov_b32 v2, 0\n"
        "v_mov_b32 v3, 1\n"
        "s_mov_b64 exec, 1\n"
        "ds_add_u32 v2, v3 offset:55332\n"
        "L_bs%=:\n"
        "ds_read_b32 v3, v2 offset:55332\n"
        "s_waitcnt lgkmcnt(0)\n"
        "v_readfirstlane_b32 s19, v3\n"
        "s_cmp_lt_u32 s19, 4\n"
        "s_cbranch_scc1 L_bs%=\n"
        "s_mov_b64 exec, -1\n"
        "ds_read2_b32 v[2:3], v172 offset0:0 offset1:32\n"
        "ds_read_b128 v[120:123], v170 offset:0\n"
        "ds_read_b128 v[126:129], v170 offset:4096\n"
        "ds_read_b128 v[104:107], v173 offset:0\n"
        "ds_read_b128 v[108:111], v173 offset:16\n"
        "ds_read_b128 v[112:115], v173 offset:8192\n"
        "ds_read_b128 v[116:119], v173 offset:8208\n"
        "s_movk_i32 s17, 4096\n"
        "s_movk_i32 s18, 8192\n"
        "s_waitcnt lgkmcnt(6)\n"
        "v_cmp_lt_u32 s[86:87], 16, v2\n"
        "v_subrev_u32 v2, 2, v2\n"
        "v_min_u32 v2, 31, v2\n"
        "s_or_b64 s[86:87], s[86:87], s[98:99]\n"
        "v_cmp_lt_u32 s[88:89], 16, v3\n"
        "v_subrev_u32 v3, 2, v3\n"
        "v_min_u32 v3, 31, v3\n"
        "s_or_b64 s[88:89], s[88:89], s[98:99]\n"
        "s_waitcnt lgkmcnt(5)\n"
        "v_cndmask_b32 v120, v120, v2, s[24:25]\n"
        "v_or_b32 v121, s94, v121\n"
        "v_or_b32 v122, s93, v122\n"
        "v_or_b32 v123, s94, v123\n"
        "v_or_b32 v120, s93, v120\n"
        "s_mov_b64 exec, s[86:87]\n"
        "buffer_store_dwordx4 v[120:123], v174, s[64:67], 0 offen sc1\n"
        "s_mov_b64 exec, -1\n"
        "s_waitcnt lgkmcnt(4)\n"
        "v_cndmask_b32 v126, v126, v3, s[24:25]\n"
        "v_or_b32 v127, s94, v127\n"
        "v_or_b32 v128, s93, v128\n"
        "v_or_b32 v129, s94, v129\n"
        "v_or_b32 v126, s93, v126\n"
        "s_mov_b64 exec, s[88:89]\n"
        "buffer_store_dwordx4 v[126:129], v174, s[64:67], s17 offen sc1\n"
        "s_mov_b64 exec, -1\n"
        "v_mov_b32 v6, s10\n"
        "s_waitcnt lgkmcnt(0)\n"
        "v_cndmask_b32 v104, v104, 0, s[24:25]\n"
        "v_cndmask_b32 v105, v105, v6, s[24:25]\n"
        "v_cndmask_b32 v112, v112, 0, s[24:25]\n"
        "v_cndmask_b32 v113, v113, v6, s[24:25]\n"
        "v_lshl_add_u32 v2, v105, 1, v104\n"
        "v_lshl_add_u32 v4, v113, 1, v112\n"
        "v_lshl_add_u32 v2, v106, 2, v2\n"
        "v_lshl_add_u32 v4, v114, 2, v4\n"
        "v_lshl_add_u32 v2, v107, 3, v2\n"
        "v_lshl_add_u32 v4, v115, 3, v4\n"
        "v_lshl_add_u32 v3, v109, 1, v108\n"
        "v_lshl_add_u32 v5, v117, 1, v116\n"
        "v_lshl_add_u32 v3, v110, 2, v3\n"
        "v_lshl_add_u32 v5, v118, 2, v5\n"
        "v_lshl_add_u32 v3, v111, 3, v3\n"
        "v_lshl_add_u32 v5, v119, 3, v5\n"
        "v_alignbit_b32 v3, v3, v3, 16\n"
        "v_alignbit_b32 v5, v5, v5, 16\n"
        "v_add_u32 v2, v2, v3\n"
        "v_add_u32 v4, v4, v5\n"
        "v_alignbit_b32 v2, v2, v2, v103\n"
        "v_alignbit_b32 v4, v4, v4, v103\n"
        "v_cndmask_b32 v2, 0, v2, s[86:87]\n"
        "v_cndmask_b32 v4, 0, v4, s[88:89]\n"
        "s_nop 0\n"
        "v_add_u32_dpp v2, v2, v2 quad_perm:[1,0,3,2] row_mask:0xf bank_mask:0xf\n"
        "v_add_u32_dpp v4, v4, v4 quad_perm:[1,0,3,2] row_mask:0xf bank_mask:0xf\n"
        "s_nop 0\n"
        "v_add_u32_dpp v2, v2, v2 quad_perm:[2,3,0,1] row_mask:0xf bank_mask:0xf\n"
        "v_add_u32_dpp v4, v4, v4 quad_perm:[2,3,0,1] row_mask:0xf bank_mask:0xf\n"
        "s_nop 0\n"
        "v_add_u32_dpp v2, v2, v2 row_half_mirror row_mask:0xf bank_mask:0xf\n"
        "v_add_u32_dpp v4, v4, v4 row_half_mirror row_mask:0xf bank_mask:0xf\n"
        "v_sub_u32 v2, 0, v2\n"
        "v_sub_u32 v4, 0, v4\n"
        "v_cndmask_b32 v104, v104, v2, s[24:25]\n"
        "v_cndmask_b32 v112, v112, v4, s[24:25]\n"
        "s_mov_b64 exec, s[86:87]\n"
        "buffer_store_dwordx4 v[104:107], v175, s[64:67], 0 offen offset:0 sc1\n"
        "buffer_store_dwordx4 v[108:111], v175, s[64:67], 0 offen offset:128 sc1\n"
        "s_mov_b64 exec, s[88:89]\n"
        "buffer_store_dwordx4 v[112:115], v175, s[64:67], s18 offen offset:0 sc1\n"
        "buffer_store_dwordx4 v[116:119], v175, s[64:67], s18 offen offset:128 sc1\n"
        "s_mov_b64 exec, -1\n"
        "v_bfe_u32 v6, v0, 3, 5\n"
        "v_add_u32 v6, s11, v6\n"
        "s_and_b32 s17, s9, 63\n"
        "s_lshl_b32 s18, s17, 7\n"
        "s_add_u32 s18, s18, 270336\n"
        "v_add_u32 v3, s18, v176\n"
        "s_movk_i32 s19, 8320\n"
        "v_mad_u32_u24 v130, v6, s19, v3\n"
        "s_lshl_b32 s18, s17, 8\n"
        "s_add_u32 s18, s18, 2400256\n"
        "v_add_u32 v3, s18, v176\n"
        "s_movk_i32 s19, 16512\n"
        "v_mad_u32_u24 v37, v6, s19, v3\n"
        "v_sub_u32 v131, 2, v103\n"
        "v_mov_b32 v102, 0\n"
        "v_and_b32 v9, 31, v1\n"
        "v_lshrrev_b32 v10, 5, v1\n"
        "v_bfe_u32 v11, v1, 2, 2\n"
        "v_and_b32 v12, 3, v1\n"
        "v_bfe_u32 v13, v1, 4, 1\n"
        "v_xor_b32 v2, s13, v11\n"
        "v_and_b32 v3, 1, v12\n"
        "v_lshl_or_b32 v3, v3, 1, v13\n"
        "v_lshl_or_b32 v2, v2, 2, v3\n"
        "v_lshlrev_b32 v3, 2, v12\n"
        "v_and_b32 v3, 8, v3\n"
        "v_lshl_or_b32 v2, v2, 4, v3\n"
        "v_lshl_or_b32 v2, v11, 8, v2\n"
        "v_add_u32 v14, 131072, v2\n"
        "v_lshl_add_u32 v15, v10, 11, v14\n"
        "v_lshrrev_b32 v2, 2, v9\n"
        "v_mul_u32_u24 v2, 1088, v2\n"
        "v_xor_b32 v3, v12, v10\n"
        "v_lshl_add_u32 v2, v3, 4, v2\n"
        "v_lshl_add_u32 v2, v12, 8, v2\n"
        "s_mul_i32 s19, s14, 8704\n"
        "s_add_u32 s19, s19, 32768\n"
        "v_add_u32 v16, s19, v2\n"
        "v_xor_b32 v17, 32, v16\n"
        "s_lshl_b32 s19, s9, 8\n"
        "s_lshl_b32 s18, s13, 6\n"
        "s_add_u32 s19, s19, s18\n"
        "s_add_u32 s19, s19, 2097152\n"
        "v_and_b32 v2, 7, v1\n"
        "v_lshl_add_u32 v2, v2, 3, s19\n"
        "v_lshrrev_b32 v3, 3, v1\n"
        "v_lshl_add_u32 v18, v3, 16, v2\n"
        "v_lshlrev_b32 v2, 6, v10\n"
        "v_lshl_or_b32 v19, v9, 7, v2\n"
        "v_lshlrev_b32 v20, 4, v1\n"
        "v_mov_b64 v[26:27], 0\n"
        "v_mov_b64 v[28:29], 0\n"
        "v_lshlrev_b32 v2, 3, v10\n"
        "v_sub_u32 v2, v9, v2\n"
        "v_mfma_f32_32x32x16_bf16 v[46:61], v[26:29], v[26:29], 0\n"
        "v_lshlrev_b32 v2, 4, v2\n"
        "v_mov_b32 v3, 0x3f80\n"
        "v_mov_b32 v5, 0\n"
        "v_cmp_gt_u32 vcc, 64, v2\n"
        "s_nop 1\n"
        "v_cndmask_b32 v4, 0, v3, vcc\n"
        "v_lshlrev_b64 v[22:23], v2, v[4:5]\n"
        "v_subrev_u32 v6, 64, v2\n"
        "v_cmp_gt_u32 vcc, 64, v6\n"
        "v_mfma_f32_32x32x16_bf16 v[62:77], v[26:29], v[26:29], 0\n"
        "s_nop 0\n"
        "v_cndmask_b32 v4, 0, v3, vcc\n"
        "v_lshlrev_b64 v[24:25], v6, v[4:5]\n"
        "v_subrev_u32 v6, 256, v2\n"
        "v_cmp_gt_u32 vcc, 64, v6\n"
        "v_mfma_f32_32x32x16_bf16 v[78:93], v[26:29], v[26:29], 0\n"
        "s_nop 0\n"
        "v_cndmask_b32 v4, 0, v3, vcc\n"
        "v_lshlrev_b64 v[26:27], v6, v[4:5]\n"
        "v_subrev_u32 v6, 320, v2\n"
        "v_cmp_gt_u32 vcc, 64, v6\n"
        "s_nop 1\n"
        "v_cndmask_b32 v4, 0, v3, vcc\n"
        "v_lshlrev_b64 v[28:29], v6, v[4:5]\n"
        "v_bfe_u32 v2, v9, 1, 2\n"
        "v_xor_b32 v2, v2, v10\n"
        "v_lshlrev_b32 v2, 4, v2\n"
        "v_lshl_or_b32 v2, v9, 7, v2\n"
        "v_add_u32 v21, s12, v2\n"
        "v_xor_b32 v30, 32, v21\n"
        "v_lshlrev_b32 v2, 4, v10\n"
        "v_lshl_or_b32 v31, v9, 5, v2\n"
        "v_lshl_add_u32 v2, v10, 2, v11\n"
        "v_add_u32 v2, v2, v13\n"
        "v_lshlrev_b32 v3, 3, v12\n"
        "v_lshl_or_b32 v32, v2, 5, v3\n"
        "v_lshrrev_b32 v2, 3, v1\n"
        "s_movk_i32 s19, 28672\n"
        "v_lshl_add_u32 v33, v2, 2, s19\n"
        "v_bfe_u32 v2, v1, 4, 2\n"
        "v_and_b32 v4, 7, v1\n"
        "v_xor_b32 v2, v2, v4\n"
        "s_lshl_b32 s19, s15, 7\n"
        "v_lshl_add_u32 v45, v2, 4, s19\n"
        "v_add_u32 v39, 3072, v45\n"
        "v_add_u32 v41, 2048, v45\n"
        "v_add_u32 v43, 1024, v45\n"
        "s_sub_u32 s68, s68, 3072\n"
        "s_subb_u32 s69, s69, 0\n"
        "s_or_b32 s69, s69, 0x1000000\n"
        "s_lshr_b32 s70, s70, 8\n"
        "v_lshl_or_b32 v3, v13, 5, v3\n"
        "v_or_b32 v3, s19, v3\n"
        "v_lshlrev_b32 v2, 6, v11\n"
        "v_xor_b32 v3, v3, v2\n"
        "v_add_u32 v34, 43776, v3\n"
        "v_cmp_eq_u32 vcc, 0, v10\n"
        "v_and_b32 v2, 1, v11\n"
        "v_or_b32 v2, 8, v2\n"
        "v_cndmask_b32 v2, v2, v11, vcc\n"
        "v_lshl_add_u32 v35, v2, 10, v34\n"
        "v_lshl_add_u32 v2, v11, 2, 16\n"
        "v_add_u32 v3, 40, v11\n"
        "v_cndmask_b32 v2, v3, v2, vcc\n"
        "v_lshl_add_u32 v36, v2, 8, v34\n"
        "v_mov_b32 v2, 0x100\n"
        "v_cndmask_b32 v34, 0, v2, vcc\n"
        "s_mov_b32 s20, 0xbfb8aa3b\n"
        "s_mov_b32 s21, 0xbfb8aa3b\n"
        "s_mov_b32 s33, 266240\n"
        "s_mov_b32 s90, 0xffc000c0\n"
        "s_mov_b32 s22, 0\n"
        "s_mov_b32 s23, 528384\n"
        "s_waitcnt vmcnt(0)\n"
        "s_setprio 2\n"
        "buffer_load_dwordx4 v[120:123], v130, s[64:67], 0 offen sc1\n"
        "buffer_load_dwordx4 v[126:129], v130, s[64:67], s33 offen sc1\n"
        "LA_chk%=:\n"
        "s_waitcnt vmcnt(0)\n"
        "v_mov_b32_dpp v132, v120 quad_perm:[0,0,0,0] row_mask:0xf bank_mask:0xf\n"
        "v_mov_b32_dpp v133, v126 quad_perm:[0,0,0,0] row_mask:0xf bank_mask:0xf\n"
        "v_bitop3_b32 v2, v120, v122, s93 bitop3:0x7e\n"
        "v_bitop3_b32 v4, v126, v128, s93 bitop3:0x7e\n"
        "v_mov_b32_dpp v132, v132 row_half_mirror row_mask:0xf bank_mask:0xa\n"
        "v_mov_b32_dpp v133, v133 row_half_mirror row_mask:0xf bank_mask:0xa\n"
        "v_bitop3_b32 v3, v121, v123, s94 bitop3:0x7e\n"
        "v_bitop3_b32 v5, v127, v129, s94 bitop3:0x7e\n"
        "v_and_b32 v132, 63, v132\n"
        "v_and_b32 v133, 63, v133\n"
        "v_bitop3_b32 v2, v2, v3, s90 bitop3:0xa8\n"
        "v_bitop3_b32 v4, v4, v5, s90 bitop3:0xa8\n"
        "v_cmp_lt_u32 s[86:87], 14, v132\n"
        "v_cmp_lt_u32 s[88:89], 14, v133\n"
        "v_cmp_ne_u32 s[26:27], 0, v2\n"
        "v_cmp_ne_u32 s[28:29], 0, v4\n"
        "s_or_b64 s[86:87], s[86:87], s[98:99]\n"
        "s_and_b64 s[26:27], s[26:27], s[86:87]\n"
        "s_or_b64 s[88:89], s[88:89], s[98:99]\n"
        "s_and_b64 s[28:29], s[28:29], s[88:89]\n"
        "s_or_b64 s[30:31], s[26:27], s[28:29]\n"
        "s_cbranch_scc0 LA_ok%=\n"
        "s_add_u32 s22, s22, 1\n"
        "s_cmp_gt_u32 s22, 8192\n"
        "s_cbranch_scc1 L_tmoa%=\n"
        "s_sleep 4\n"
        "s_mov_b64 exec, s[26:27]\n"
        "buffer_load_dwordx4 v[120:123], v130, s[64:67], 0 offen sc1\n"
        "s_mov_b64 exec, s[28:29]\n"
        "buffer_load_dwordx4 v[126:129], v130, s[64:67], s33 offen sc1\n"
        "s_mov_b64 exec, -1\n"
        "s_branch LA_chk%=\n"
        "L_tmoa%=:\n"
        "v_mov_b32 v2, 0\n"
        "v_mov_b32 v3, 1\n"
        "ds_write_b32 v2, v3 offset:55312\n"
        "ds_write_b32 v2, v3 offset:55316\n"
        "s_waitcnt lgkmcnt(0)\n"
        "s_barrier\n"
        "s_branch L_join5%=\n"
        "LA_ok%=:\n"
        "s_mov_b64 exec, s[86:87]\n"
        "buffer_load_dwordx4 v[104:107], v37, s[64:67], 0 offen offset:0 sc1\n"
        "buffer_load_dwordx4 v[108:111], v37, s[64:67], 0 offen offset:128 sc1\n"
        "s_mov_b64 exec, s[88:89]\n"
        "buffer_load_dwordx4 v[112:115], v37, s[64:67], s23 offen offset:0 sc1\n"
        "buffer_load_dwordx4 v[116:119], v37, s[64:67], s23 offen offset:128 sc1\n"
        "s_mov_b64 exec, -1\n"
        "v_max_u32 v2, v132, v133\n"
        "v_cmp_lt_u32 vcc, 30, v2\n"
        "v_add_u32 v132, v132, v131\n"
        "v_add_u32 v133, v133, v131\n"
        "v_mov_b32 v5, 32\n"
        "v_cmp_lt_i32 s[26:27], 0, v132\n"
        "v_lshlrev_b32 v134, 2, v120\n"
        "v_and_b32 v134, 12, v134\n"
        "v_cmp_lt_i32 s[28:29], 1, v132\n"
        "v_lshlrev_b32 v135, 2, v121\n"
        "v_and_b32 v135, 12, v135\n"
        "v_cmp_lt_i32 s[30:31], 2, v132\n"
        "v_lshlrev_b32 v136, 2, v122\n"
        "v_and_b32 v136, 12, v136\n"
        "v_cmp_lt_i32 s[34:35], 3, v132\n"
        "v_lshlrev_b32 v137, 2, v123\n"
        "v_and_b32 v137, 12, v137\n"
        "v_cmp_lt_i32 s[74:75], 0, v133\n"
        "v_lshlrev_b32 v138, 2, v126\n"
        "v_and_b32 v138, 12, v138\n"
        "v_cmp_lt_i32 s[76:77], 1, v133\n"
        "v_lshlrev_b32 v139, 2, v127\n"
        "v_and_b32 v139, 12, v139\n"
        "v_cmp_lt_i32 s[78:79], 2, v133\n"
        "v_lshlrev_b32 v140, 2, v128\n"
        "v_and_b32 v140, 12, v140\n"
        "v_cmp_lt_i32 s[80:81], 3, v133\n"
        "v_lshlrev_b32 v141, 2, v129\n"
        "v_and_b32 v141, 12, v141\n"
        "s_andn2_b64 s[26:27], s[26:27], s[24:25]\n"
        "s_andn2_b64 s[28:29], s[28:29], s[24:25]\n"
        "s_andn2_b64 s[74:75], s[74:75], s[24:25]\n"
        "s_andn2_b64 s[76:77], s[76:77], s[24:25]\n"
        "s_mov_b64 exec, s[26:27]\n"
        "ds_add_rtn_u32 v94, v134, v5 offset:55296\n"
        "s_mov_b64 exec, s[28:29]\n"
        "ds_add_rtn_u32 v95, v135, v5 offset:55296\n"
        "s_mov_b64 exec, s[30:31]\n"
        "ds_add_rtn_u32 v96, v136, v5 offset:55296\n"
        "s_mov_b64 exec, s[34:35]\n"
        "ds_add_rtn_u32 v97, v137, v5 offset:55296\n"
        "s_mov_b64 exec, s[74:75]\n"
        "ds_add_rtn_u32 v98, v138, v5 offset:55296\n"
        "s_mov_b64 exec, s[76:77]\n"
        "ds_add_rtn_u32 v99, v139, v5 offset:55296\n"
        "s_mov_b64 exec, s[78:79]\n"
        "ds_add_rtn_u32 v100, v140, v5 offset:55296\n"
        "s_mov_b64 exec, s[80:81]\n"
        "ds_add_rtn_u32 v101, v141, v5 offset:55296\n"
        "s_mov_b64 exec, vcc\n"
        "v_mov_b32 v2, 0\n"
        "ds_write_b32 v2, v171 offset:55312\n"
        "s_mov_b64 exec, -1\n"
        "v_mov_b32 v7, 0x3f80\n"
        "v_bfe_u32 v134, v120, 2, 4\n"
        "v_bfe_u32 v120, v120, 8, 14\n"
        "v_bfe_u32 v135, v121, 2, 4\n"
        "v_bfe_u32 v121, v121, 8, 14\n"
        "v_bfe_u32 v136, v122, 2, 4\n"
        "v_bfe_u32 v122, v122, 8, 14\n"
        "v_bfe_u32 v137, v123, 2, 4\n"
        "v_bfe_u32 v123, v123, 8, 14\n"
        "v_bfe_u32 v138, v126, 2, 4\n"
        "v_bfe_u32 v126, v126, 8, 14\n"
        "v_bfe_u32 v139, v127, 2, 4\n"
        "v_bfe_u32 v127, v127, 8, 14\n"
        "v_bfe_u32 v140, v128, 2, 4\n"
        "v_bfe_u32 v128, v128, 8, 14\n"
        "v_bfe_u32 v141, v129, 2, 4\n"
        "v_bfe_u32 v129, v129, 8, 14\n"
        "s_waitcnt lgkmcnt(0)\n"
        "v_min_u32 v94, 28672, v94\n"
        "v_lshrrev_b32 v142, 3, v94\n"
        "v_lshl_add_u32 v134, v134, 1, v94\n"
        "v_min_u32 v95, 28672, v95\n"
        "v_lshrrev_b32 v143, 3, v95\n"
        "v_lshl_add_u32 v135, v135, 1, v95\n"
        "v_min_u32 v96, 28672, v96\n"
        "v_lshrrev_b32 v144, 3, v96\n"
        "v_lshl_add_u32 v136, v136, 1, v96\n"
        "v_min_u32 v97, 28672, v97\n"
        "v_lshrrev_b32 v145, 3, v97\n"
        "v_lshl_add_u32 v137, v137, 1, v97\n"
        "v_min_u32 v98, 28672, v98\n"
        "v_lshrrev_b32 v146, 3, v98\n"
        "v_lshl_add_u32 v138, v138, 1, v98\n"
        "v_min_u32 v99, 28672, v99\n"
        "v_lshrrev_b32 v147, 3, v99\n"
        "v_lshl_add_u32 v139, v139, 1, v99\n"
        "v_min_u32 v100, 28672, v100\n"
        "v_lshrrev_b32 v148, 3, v100\n"
        "v_lshl_add_u32 v140, v140, 1, v100\n"
        "v_min_u32 v101, 28672, v101\n"
        "v_lshrrev_b32 v149, 3, v101\n"
        "v_lshl_add_u32 v141, v141, 1, v101\n"
        "s_mov_b64 exec, s[26:27]\n"
        "ds_write_b32 v142, v120 offset:28672\n"
        "ds_write_b16 v134, v7\n"
        "s_mov_b64 exec, s[28:29]\n"
        "ds_write_b32 v143, v121 offset:28672\n"
        "ds_write_b16 v135, v7\n"
        "s_mov_b64 exec, s[30:31]\n"
        "ds_write_b32 v144, v122 offset:28672\n"
        "ds_write_b16 v136, v7\n"
        "s_mov_b64 exec, s[34:35]\n"
        "ds_write_b32 v145, v123 offset:28672\n"
        "ds_write_b16 v137, v7\n"
        "s_mov_b64 exec, s[74:75]\n"
        "ds_write_b32 v146, v126 offset:28672\n"
        "ds_write_b16 v138, v7\n"
        "s_mov_b64 exec, s[76:77]\n"
        "ds_write_b32 v147, v127 offset:28672\n"
        "ds_write_b16 v139, v7\n"
        "s_mov_b64 exec, s[78:79]\n"
        "ds_write_b32 v148, v128 offset:28672\n"
        "ds_write_b16 v140, v7\n"
        "s_mov_b64 exec, s[80:81]\n"
        "ds_write_b32 v149, v129 offset:28672\n"
        "ds_write_b16 v141, v7\n"
        "s_mov_b64 exec, -1\n"
        "s_waitcnt lgkmcnt(0)\n"
        "s_barrier\n"
        "LB_chk%=:\n"
        "s_waitcnt vmcnt(0)\n"
        "v_lshl_add_u32 v2, v105, 1, v104\n"
        "v_lshl_add_u32 v4, v113, 1, v112\n"
        "v_lshl_add_u32 v2, v106, 2, v2\n"
        "v_lshl_add_u32 v4, v114, 2, v4\n"
        "v_lshl_add_u32 v2, v107, 3, v2\n"
        "v_lshl_add_u32 v4, v115, 3, v4\n"
        "v_lshl_add_u32 v3, v109, 1, v108\n"
        "v_lshl_add_u32 v5, v117, 1, v116\n"
        "v_lshl_add_u32 v3, v110, 2, v3\n"
        "v_lshl_add_u32 v5, v118, 2, v5\n"
        "v_lshl_add_u32 v3, v111, 3, v3\n"
        "v_lshl_add_u32 v5, v119, 3, v5\n"
        "v_alignbit_b32 v3, v3, v3, 16\n"
        "v_alignbit_b32 v5, v5, v5, 16\n"
        "v_add_u32 v2, v2, v3\n"
        "v_add_u32 v4, v4, v5\n"
        "v_alignbit_b32 v2, v2, v2, v103\n"
        "v_alignbit_b32 v4, v4, v4, v103\n"
        "v_cndmask_b32 v2, 0, v2, s[86:87]\n"
        "v_cndmask_b32 v4, 0, v4, s[88:89]\n"
        "s_nop 0\n"
        "v_add_u32_dpp v2, v2, v2 quad_perm:[1,0,3,2] row_mask:0xf bank_mask:0xf\n"
        "v_add_u32_dpp v4, v4, v4 quad_perm:[1,0,3,2] row_mask:0xf bank_mask:0xf\n"
        "s_nop 0\n"
        "v_add_u32_dpp v2, v2, v2 quad_perm:[2,3,0,1] row_mask:0xf bank_mask:0xf\n"
        "v_add_u32_dpp v4, v4, v4 quad_perm:[2,3,0,1] row_mask:0xf bank_mask:0xf\n"
        "s_nop 0\n"
        "v_add_u32_dpp v2, v2, v2 row_half_mirror row_mask:0xf bank_mask:0xf\n"
        "v_add_u32_dpp v4, v4, v4 row_half_mirror row_mask:0xf bank_mask:0xf\n"
        "v_xor_b32 v3, s10, v105\n"
        "v_xor_b32 v5, s10, v113\n"
        "v_cndmask_b32 v3, 0, v3, s[24:25]\n"
        "v_cndmask_b32 v5, 0, v5, s[24:25]\n"
        "v_or3_b32 v2, v2, v3, v4\n"
        "v_or_b32 v2, v2, v5\n"
        "v_cmp_ne_u32 vcc, 0, v2\n"
        "s_cmp_eq_u64 vcc, 0\n"
        "s_cbranch_scc1 LB_ok%=\n"
        "s_add_u32 s22, s22, 1\n"
        "s_cmp_gt_u32 s22, 8192\n"
        "s_cbranch_scc1 L_tmob%=\n"
        "s_mov_b64 exec, s[86:87]\n"
        "buffer_load_dwordx4 v[104:107], v37, s[64:67], 0 offen offset:0 sc1\n"
        "buffer_load_dwordx4 v[108:111], v37, s[64:67], 0 offen offset:128 sc1\n"
        "s_mov_b64 exec, s[88:89]\n"
        "buffer_load_dwordx4 v[112:115], v37, s[64:67], s23 offen offset:0 sc1\n"
        "buffer_load_dwordx4 v[116:119], v37, s[64:67], s23 offen offset:128 sc1\n"
        "s_mov_b64 exec, -1\n"
        "s_branch LB_chk%=\n"
        "L_tmob%=:\n"
        "v_mov_b32 v2, 0\n"
        "v_mov_b32 v3, 1\n"
        "ds_write_b32 v2, v3 offset:55312\n"
        "LB_ok%=:\n"
        "s_mov_b64 exec, s[26:27]\n"
        "ds_write_b64 v94, v[104:105] offset:24\n"
        "s_mov_b64 exec, s[28:29]\n"
        "ds_write_b64 v95, v[106:107] offset:24\n"
        "s_mov_b64 exec, s[30:31]\n"
        "ds_write_b64 v96, v[108:109] offset:24\n"
        "s_mov_b64 exec, s[34:35]\n"
        "ds_write_b64 v97, v[110:111] offset:24\n"
        "s_mov_b64 exec, s[74:75]\n"
        "ds_write_b64 v98, v[112:113] offset:24\n"
        "s_mov_b64 exec, s[76:77]\n"
        "ds_write_b64 v99, v[114:115] offset:24\n"
        "s_mov_b64 exec, s[78:79]\n"
        "ds_write_b64 v100, v[116:117] offset:24\n"
        "s_mov_b64 exec, s[80:81]\n"
        "ds_write_b64 v101, v[118:119] offset:24\n"
        "s_mov_b64 exec, -1\n"
        "L_join5%=:\n"
        "s_waitcnt lgkmcnt(0)\n"
        "s_barrier\n"
        "s_branch L_asg%=\n"
        "L_join%=:\n"
        "s_setprio 3\n"
        "s_waitcnt lgkmcnt(0)\n"
        "s_barrier\n"
        "L_asg%=:\n"
        "ds_read_b128 v[94:97], v102 offset:55296\n"
        "s_bfe_u32 s18, s8, 0x10001\n"
        "s_sub_u32 s19, 3, s18\n"
        "s_cmp_ge_u32 s8, 4\n"
        "s_cselect_b32 s18, s19, s18\n"
        "s_mov_b32 m0, s12\n"
        "s_waitcnt lgkmcnt(0)\n"
        "v_readfirstlane_b32 s33, v94\n"
        "v_readfirstlane_b32 s90, v95\n"
        "v_readfirstlane_b32 s91, v96\n"
        "v_readfirstlane_b32 s92, v97\n"
        "s_sub_u32 s90, s90, 7167\n"
        "s_sub_u32 s91, s91, 14334\n"
        "s_sub_u32 s92, s92, 21501\n"
        "s_max_u32 s93, s33, s90\n"
        "s_min_u32 s90, s33, s90\n"
        "s_max_u32 s33, s91, s92\n"
        "s_min_u32 s92, s91, s92\n"
        "s_max_u32 s91, s93, s33\n"
        "s_min_u32 s33, s93, s33\n"
        "s_max_u32 s93, s90, s92\n"
        "s_min_u32 s92, s90, s92\n"
        "s_max_u32 s90, s93, s33\n"
        "s_min_u32 s33, s93, s33\n"
        "s_bitcmp1_b32 s18, 0\n"
        "s_cselect_b32 s93, s90, s91\n"
        "s_cselect_b32 s33, s92, s33\n"
        "s_bitcmp1_b32 s18, 1\n"
        "s_cselect_b32 s93, s33, s93\n"
        "s_and_b32 s90, s93, 3\n"
        "s_mul_i32 s17, s90, 896\n"
        "v_add_u32 v33, s17, v33\n"
        "ds_read_b32 v38, v33 offset:0\n"
        "ds_read_b32 v40, v33 offset:32\n"
        "ds_read_b32 v42, v33 offset:64\n"
        "ds_read_b32 v44, v33 offset:96\n"
        "s_cmp_gt_u32 s91, 7199\n"
        "s_cselect_b32 s94, 1, 0\n"
        "s_lshl_b32 s17, s17, 3\n"
        "v_add_u32 v31, s17, v31\n"
        "v_add_u32 v32, s17, v32\n"
        "s_lshl_b32 s17, s90, 8\n"
        "v_add_u32 v35, s17, v35\n"
        "v_mad_u32_u24 v36, v34, s90, v36\n"
        "s_waitcnt lgkmcnt(3)\n"
        "buffer_load_dwordx4 v[38:39], s[68:71], 0 idxen offen offset:0 sc1 lds\n"
        "s_waitcnt lgkmcnt(2)\n"
        "buffer_load_dwordx4 v[40:41], s[68:71], 0 idxen offen offset:1024 sc1 lds\n"
        "s_waitcnt lgkmcnt(1)\n"
        "buffer_load_dwordx4 v[42:43], s[68:71], 0 idxen offen offset:2048 sc1 lds\n"
        "s_waitcnt lgkmcnt(0)\n"
        "buffer_load_dwordx4 v[44:45], s[68:71], 0 idxen offen offset:3072 sc1 lds\n"
        "ds_read_b32 v38, v33 offset:128\n"
        "ds_read_b32 v40, v33 offset:160\n"
        "ds_read_b32 v42, v33 offset:192\n"
        "ds_read_b32 v44, v33 offset:224\n"
        "v_xor_b32 v4, 64, v35\n"
        "v_xor_b32 v5, 64, v36\n"
        "ds_read_b64_tr_b16 v[94:95], v35\n"
        "ds_read_b64_tr_b16 v[96:97], v36\n"
        "ds_read_b64_tr_b16 v[98:99], v4\n"
        "ds_read_b64_tr_b16 v[100:101], v5\n"
        "s_add_u32 s17, s93, 992\n"
        "s_lshr_b32 s92, s17, 10\n"
        "s_bfe_u32 s33, s17, 0x20008\n"
        "s_max_u32 s92, s92, 1\n"
        "v_mov_b32 v2, 0\n"
        "s_setprio 0\n"
        "s_xor_b32 s95, s12, 0x8000\n"
        "s_waitcnt lgkmcnt(3)\n"
        "ds_read_b64_tr_b16 v[152:153], v15 offset:0\n"
        "ds_read_b64_tr_b16 v[154:155], v15 offset:1024\n"
        "ds_read_b128 v[120:123], v16 offset:0\n"
        "ds_read_b64_tr_b16 v[156:157], v15 offset:4096\n"
        "ds_read_b64_tr_b16 v[158:159], v15 offset:5120\n"
        "ds_read_b128 v[124:127], v17 offset:0\n"
        "ds_read_b64_tr_b16 v[160:161], v15 offset:8192\n"
        "ds_read_b64_tr_b16 v[162:163], v15 offset:9216\n"
        "ds_read_b128 v[128:131], v16 offset:64\n"
        "ds_read_b64_tr_b16 v[164:165], v15 offset:12288\n"
        "ds_read_b64_tr_b16 v[166:167], v15 offset:13312\n"
        "ds_read_b128 v[132:135], v17 offset:64\n"
        "s_waitcnt lgkmcnt(9)\n"
        "v_mfma_f32_32x32x16_bf16 v[104:119], v[152:155], v[120:123], 0\n"
        "ds_read_b64_tr_b16 v[168:169], v15 offset:16384\n"
        "ds_read_b64_tr_b16 v[170:171], v15 offset:17408\n"
        "ds_read_b128 v[136:139], v16 offset:128\n"
        "s_waitcnt lgkmcnt(9)\n"
        "v_mfma_f32_32x32x16_bf16 v[104:119], v[156:159], v[124:127], v[104:119]\n"
        "ds_read_b64_tr_b16 v[172:173], v15 offset:20480\n"
        "ds_read_b64_tr_b16 v[174:175], v15 offset:21504\n"
        "ds_read_b128 v[140:143], v17 offset:128\n"
        "s_waitcnt lgkmcnt(9)\n"
        "v_mfma_f32_32x32x16_bf16 v[104:119], v[160:163], v[128:131], v[104:119]\n"
        "ds_read_b64_tr_b16 v[176:177], v15 offset:24576\n"
        "ds_read_b64_tr_b16 v[178:179], v15 offset:25600\n"
        "ds_read_b128 v[144:147], v16 offset:192\n"
        "s_waitcnt lgkmcnt(9)\n"
        "v_mfma_f32_32x32x16_bf16 v[104:119], v[164:167], v[132:135], v[104:119]\n"
        "ds_read_b64_tr_b16 v[180:181], v15 offset:28672\n"
        "ds_read_b64_tr_b16 v[182:183], v15 offset:29696\n"
        "ds_read_b128 v[148:151], v17 offset:192\n"
        "s_waitcnt lgkmcnt(9)\n"
        "v_mfma_f32_32x32x16_bf16 v[104:119], v[168:171], v[136:139], v[104:119]\n"
        "s_waitcnt lgkmcnt(6)\n"
        "v_mfma_f32_32x32x16_bf16 v[104:119], v[172:175], v[140:143], v[104:119]\n"
        "s_waitcnt lgkmcnt(3)\n"
        "v_mfma_f32_32x32x16_bf16 v[104:119], v[176:179], v[144:147], v[104:119]\n"
        "s_waitcnt lgkmcnt(0)\n"
        "v_mfma_f32_32x32x16_bf16 v[104:119], v[180:183], v[148:151], v[104:119]\n"
        "v_mov_b32 v3, 1\n"
        "s_mov_b64 exec, 1\n"
        "ds_add_u32 v2, v3 offset:55328\n"
        "s_mov_b64 exec, -1\n"
        "s_cmp_ge_u32 s8, 4\n"
        "s_cbranch_scc1 L_nob5%=\n"
        "s_waitcnt lgkmcnt(0)\n"
        "s_barrier\n"
        "L_nob5%=:\n"
        "ds_read_b64 v[4:5], v2 offset:55312\n"
        "s_mov_b32 m0, s95\n"
        "s_nop 0\n"
        "buffer_load_dwordx4 v[38:39], s[68:71], 0 idxen offen offset:0 sc1 lds\n"
        "buffer_load_dwordx4 v[40:41], s[68:71], 0 idxen offen offset:1024 sc1 lds\n"
        "buffer_load_dwordx4 v[42:43], s[68:71], 0 idxen offen offset:2048 sc1 lds\n"
        "buffer_load_dwordx4 v[44:45], s[68:71], 0 idxen offen offset:3072 sc1 lds\n"
        "s_waitcnt lgkmcnt(0)\n"
        "ds_read_b128 v[172:175], v31\n"
        "v_readfirstlane_b32 s16, v4\n"
        "v_readfirstlane_b32 s17, v5\n"
        "s_or_b32 s16, s16, s94\n"
        "s_cmp_eq_u32 %2, 0\n"
        "s_cselect_b32 s17, 1, s17\n"
        "s_cmp_eq_u32 s8, 0\n"
        "s_cselect_b32 s17, s17, 0\n"
        "s_cmp_eq_u32 s17, 0\n"
        "s_cbranch_scc1 L_nocas%=\n"
        "s_sub_u32 s17, s10, 0x400001\n"
        "v_mov_b32 v4, s10\n"
        "v_mov_b32 v5, s17\n"
        "s_waitcnt vmcnt(0)\n"
        "s_mov_b64 exec, 1\n"
        "buffer_atomic_cmpswap v[4:5], off, s[64:67], 0 sc1\n"
        "s_mov_b64 exec, -1\n"
        "L_nocas%=:\n"
        "s_mov_b32 %0, s16\n"
        "s_cmp_lg_u32 s16, 0\n"
        "s_cbranch_scc1 L_end%=\n"
        "s_waitcnt lgkmcnt(0)\n"
        "v_mfma_f32_32x32x16_bf16 v[120:135], v[172:175], v[94:97], 0\n"
        "s_waitcnt vmcnt(4)\n"
        "ds_read_b128 v[34:37], v21 offset:0\n"
        "ds_read_b128 v[168:171], v30 offset:0\n"
        "s_waitcnt lgkmcnt(0)\n"
        "v_mfma_f32_32x32x16_bf16 v[120:135], v[34:37], v[22:25], v[120:135]\n"
        "v_mfma_f32_32x32x16_bf16 v[120:135], v[168:171], v[26:29], v[120:135]\n"
        "s_cmp_eq_u32 s92, 1\n"
        "s_cbranch_scc1 L_tail%=\n"
        "s_setprio 3\n"
        "s_nop 7\n"
        "L_loop%=:\n"
        "v_mfma_f32_32x32x16_bf16 v[62:77], v[78:81], v[86:89], v[62:77]\n"
        "v_exp_f32 v152, v120\n"
        "v_exp_f32 v153, v121\n"
        "v_exp_f32 v154, v122\n"
        "v_mfma_f32_32x32x16_bf16 v[62:77], v[82:85], v[90:93], v[62:77]\n"
        "v_exp_f32 v155, v123\n"
        "v_exp_f32 v156, v124\n"
        "v_exp_f32 v157, v125\n"
        "v_exp_f32 v158, v126\n"
        "v_exp_f32 v159, v127\n"
        "v_exp_f32 v160, v128\n"
        "v_exp_f32 v161, v129\n"
        "ds_read_b128 v[34:37], v21 offset:64\n"
        "ds_read_b128 v[168:171], v30 offset:64\n"
        "ds_read_b32 v38, v33 offset:256\n"
        "ds_read_b32 v40, v33 offset:288\n"
        "ds_read_b32 v42, v33 offset:320\n"
        "ds_read_b32 v44, v33 offset:352\n"
        "ds_read_b64_tr_b16 v[86:87], v32 offset:0\n"
        "ds_read_b64_tr_b16 v[88:89], v32 offset:256\n"
        "ds_read_b64_tr_b16 v[90:91], v32 offset:512\n"
        "ds_read_b64_tr_b16 v[92:93], v32 offset:768\n"
        "v_exp_f32 v162, v130\n"
        "v_exp_f32 v163, v131\n"
        "v_exp_f32 v164, v132\n"
        "v_exp_f32 v165, v133\n"
        "v_exp_f32 v166, v134\n"
        "v_exp_f32 v167, v135\n"
        "v_pk_fma_f32 v[152:153], v[152:153], s[20:21], s[20:21]\n"
        "v_pk_fma_f32 v[154:155], v[154:155], s[20:21], s[20:21]\n"
        "v_pk_fma_f32 v[156:157], v[156:157], s[20:21], s[20:21]\n"
        "v_pk_fma_f32 v[158:159], v[158:159], s[20:21], s[20:21]\n"
        "v_pk_fma_f32 v[160:161], v[160:161], s[20:21], s[20:21]\n"
        "v_pk_fma_f32 v[162:163], v[162:163], s[20:21], s[20:21]\n"
        "v_pk_fma_f32 v[164:165], v[164:165], s[20:21], s[20:21]\n"
        "v_pk_fma_f32 v[166:167], v[166:167], s[20:21], s[20:21]\n"
        "s_waitcnt lgkmcnt(0)\n"
        "v_mfma_f32_32x32x16_bf16 v[136:151], v[34:37], v[22:25], 0\n"
        "v_rcp_f32 v152, v152\n"
        "v_rcp_f32 v153, v153\n"
        "v_rcp_f32 v154, v154\n"
        "v_mfma_f32_32x32x16_bf16 v[136:151], v[168:171], v[26:29], v[136:151]\n"
        "v_rcp_f32 v155, v155\n"
        "v_rcp_f32 v156, v156\n"
        "v_rcp_f32 v157, v157\n"
        "v_mfma_f32_32x32x16_bf16 v[136:151], v[172:175], v[98:101], v[136:151]\n"
        "v_rcp_f32 v158, v158\n"
        "v_rcp_f32 v159, v159\n"
        "v_rcp_f32 v160, v160\n"
        "v_rcp_f32 v161, v161\n"
        "v_rcp_f32 v162, v162\n"
        "v_rcp_f32 v163, v163\n"
        "v_rcp_f32 v164, v164\n"
        "v_rcp_f32 v165, v165\n"
        "v_rcp_f32 v166, v166\n"
        "v_rcp_f32 v167, v167\n"
        "v_pk_mul_f32 v[152:153], v[152:153], v[120:121]\n"
        "v_pk_mul_f32 v[154:155], v[154:155], v[122:123]\n"
        "v_pk_mul_f32 v[156:157], v[156:157], v[124:125]\n"
        "v_pk_mul_f32 v[158:159], v[158:159], v[126:127]\n"
        "v_pk_mul_f32 v[160:161], v[160:161], v[128:129]\n"
        "v_pk_mul_f32 v[162:163], v[162:163], v[130:131]\n"
        "v_pk_mul_f32 v[164:165], v[164:165], v[132:133]\n"
        "v_pk_mul_f32 v[166:167], v[166:167], v[134:135]\n"
        "v_cvt_pk_bf16_f32 v78, v152, v153\n"
        "v_cvt_pk_bf16_f32 v79, v154, v155\n"
        "v_cvt_pk_bf16_f32 v80, v156, v157\n"
        "v_cvt_pk_bf16_f32 v81, v158, v159\n"
        "v_cvt_pk_bf16_f32 v82, v160, v161\n"
        "v_cvt_pk_bf16_f32 v83, v162, v163\n"
        "v_cvt_pk_bf16_f32 v84, v164, v165\n"
        "v_cvt_pk_bf16_f32 v85, v166, v167\n"
        "v_mfma_f32_32x32x16_bf16 v[46:61], v[78:81], v[86:89], v[46:61]\n"
        "s_waitcnt vmcnt(0)\n"
        "v_exp_f32 v152, v136\n"
        "v_exp_f32 v153, v137\n"
        "v_exp_f32 v154, v138\n"
        "v_mfma_f32_32x32x16_bf16 v[46:61], v[82:85], v[90:93], v[46:61]\n"
        "v_exp_f32 v155, v139\n"
        "v_exp_f32 v156, v140\n"
        "v_exp_f32 v157, v141\n"
        "v_exp_f32 v158, v142\n"
        "v_exp_f32 v159, v143\n"
        "v_exp_f32 v160, v144\n"
        "v_exp_f32 v161, v145\n"
        "s_mov_b32 m0, s12\n"
        "ds_read_b128 v[34:37], v21 offset:32768\n"
        "ds_read_b128 v[168:171], v30 offset:32768\n"
        "ds_read_b128 v[172:175], v31 offset:1024\n"
        "buffer_load_dwordx4 v[38:39], s[68:71], 0 idxen offen offset:0 sc1 lds\n"
        "buffer_load_dwordx4 v[40:41], s[68:71], 0 idxen offen offset:1024 sc1 lds\n"
        "buffer_load_dwordx4 v[42:43], s[68:71], 0 idxen offen offset:2048 sc1 lds\n"
        "buffer_load_dwordx4 v[44:45], s[68:71], 0 idxen offen offset:3072 sc1 lds\n"
        "s_sub_u32 s92, s92, 1\n"
        "s_cmp_gt_u32 s92, 2\n"
        "s_cbranch_scc1 L_p30%=\n"
        "s_setprio 2\n"
        "L_p30%=:\n"
        "v_exp_f32 v162, v146\n"
        "v_exp_f32 v163, v147\n"
        "v_exp_f32 v164, v148\n"
        "v_exp_f32 v165, v149\n"
        "v_exp_f32 v166, v150\n"
        "v_exp_f32 v167, v151\n"
        "v_pk_fma_f32 v[152:153], v[152:153], s[20:21], s[20:21]\n"
        "v_pk_fma_f32 v[154:155], v[154:155], s[20:21], s[20:21]\n"
        "v_pk_fma_f32 v[156:157], v[156:157], s[20:21], s[20:21]\n"
        "v_pk_fma_f32 v[158:159], v[158:159], s[20:21], s[20:21]\n"
        "v_pk_fma_f32 v[160:161], v[160:161], s[20:21], s[20:21]\n"
        "v_pk_fma_f32 v[162:163], v[162:163], s[20:21], s[20:21]\n"
        "v_pk_fma_f32 v[164:165], v[164:165], s[20:21], s[20:21]\n"
        "v_pk_fma_f32 v[166:167], v[166:167], s[20:21], s[20:21]\n"
        "s_waitcnt lgkmcnt(0)\n"
        "v_mfma_f32_32x32x16_bf16 v[120:135], v[34:37], v[22:25], 0\n"
        "v_rcp_f32 v152, v152\n"
        "v_rcp_f32 v153, v153\n"
        "v_rcp_f32 v154, v154\n"
        "v_mfma_f32_32x32x16_bf16 v[120:135], v[168:171], v[26:29], v[120:135]\n"
        "v_rcp_f32 v155, v155\n"
        "v_rcp_f32 v156, v156\n"
        "v_rcp_f32 v157, v157\n"
        "v_mfma_f32_32x32x16_bf16 v[120:135], v[172:175], v[94:97], v[120:135]\n"
        "v_rcp_f32 v158, v158\n"
        "v_rcp_f32 v159, v159\n"
        "v_rcp_f32 v160, v160\n"
        "v_rcp_f32 v161, v161\n"
        "v_rcp_f32 v162, v162\n"
        "v_rcp_f32 v163, v163\n"
        "v_rcp_f32 v164, v164\n"
        "v_rcp_f32 v165, v165\n"
        "v_rcp_f32 v166, v166\n"
        "v_rcp_f32 v167, v167\n"
        "v_pk_mul_f32 v[152:153], v[152:153], v[136:137]\n"
        "v_pk_mul_f32 v[154:155], v[154:155], v[138:139]\n"
        "v_pk_mul_f32 v[156:157], v[156:157], v[140:141]\n"
        "v_pk_mul_f32 v[158:159], v[158:159], v[142:143]\n"
        "v_pk_mul_f32 v[160:161], v[160:161], v[144:145]\n"
        "v_pk_mul_f32 v[162:163], v[162:163], v[146:147]\n"
        "v_pk_mul_f32 v[164:165], v[164:165], v[148:149]\n"
        "v_pk_mul_f32 v[166:167], v[166:167], v[150:151]\n"
        "v_cvt_pk_bf16_f32 v78, v152, v153\n"
        "v_cvt_pk_bf16_f32 v79, v154, v155\n"
        "v_cvt_pk_bf16_f32 v80, v156, v157\n"
        "v_cvt_pk_bf16_f32 v81, v158, v159\n"
        "v_cvt_pk_bf16_f32 v82, v160, v161\n"
        "v_cvt_pk_bf16_f32 v83, v162, v163\n"
        "v_cvt_pk_bf16_f32 v84, v164, v165\n"
        "v_cvt_pk_bf16_f32 v85, v166, v167\n"
        "s_cmp_gt_u32 s92, 1\n"
        "s_cbranch_scc0 L_odd%=\n"
        "v_mfma_f32_32x32x16_bf16 v[62:77], v[78:81], v[86:89], v[62:77]\n"
        "v_exp_f32 v152, v120\n"
        "v_exp_f32 v153, v121\n"
        "v_exp_f32 v154, v122\n"
        "v_mfma_f32_32x32x16_bf16 v[62:77], v[82:85], v[90:93], v[62:77]\n"
        "v_exp_f32 v155, v123\n"
        "v_exp_f32 v156, v124\n"
        "v_exp_f32 v157, v125\n"
        "v_exp_f32 v158, v126\n"
        "v_exp_f32 v159, v127\n"
        "v_exp_f32 v160, v128\n"
        "v_exp_f32 v161, v129\n"
        "ds_read_b128 v[34:37], v21 offset:32832\n"
        "ds_read_b128 v[168:171], v30 offset:32832\n"
        "ds_read_b32 v38, v33 offset:384\n"
        "ds_read_b32 v40, v33 offset:416\n"
        "ds_read_b32 v42, v33 offset:448\n"
        "ds_read_b32 v44, v33 offset:480\n"
        "ds_read_b64_tr_b16 v[86:87], v32 offset:1024\n"
        "ds_read_b64_tr_b16 v[88:89], v32 offset:1280\n"
        "ds_read_b64_tr_b16 v[90:91], v32 offset:1536\n"
        "ds_read_b64_tr_b16 v[92:93], v32 offset:1792\n"
        "v_exp_f32 v162, v130\n"
        "v_exp_f32 v163, v131\n"
        "v_exp_f32 v164, v132\n"
        "v_exp_f32 v165, v133\n"
        "v_exp_f32 v166, v134\n"
        "v_exp_f32 v167, v135\n"
        "v_pk_fma_f32 v[152:153], v[152:153], s[20:21], s[20:21]\n"
        "v_pk_fma_f32 v[154:155], v[154:155], s[20:21], s[20:21]\n"
        "v_pk_fma_f32 v[156:157], v[156:157], s[20:21], s[20:21]\n"
        "v_pk_fma_f32 v[158:159], v[158:159], s[20:21], s[20:21]\n"
        "v_pk_fma_f32 v[160:161], v[160:161], s[20:21], s[20:21]\n"
        "v_pk_fma_f32 v[162:163], v[162:163], s[20:21], s[20:21]\n"
        "v_pk_fma_f32 v[164:165], v[164:165], s[20:21], s[20:21]\n"
        "v_pk_fma_f32 v[166:167], v[166:167], s[20:21], s[20:21]\n"
        "s_waitcnt lgkmcnt(0)\n"
        "v_mfma_f32_32x32x16_bf16 v[136:151], v[34:37], v[22:25], 0\n"
        "v_rcp_f32 v152, v152\n"
        "v_rcp_f32 v153, v153\n"
        "v_rcp_f32 v154, v154\n"
        "v_mfma_f32_32x32x16_bf16 v[136:151], v[168:171], v[26:29], v[136:151]\n"
        "v_rcp_f32 v155, v155\n"
        "v_rcp_f32 v156, v156\n"
        "v_rcp_f32 v157, v157\n"
        "v_mfma_f32_32x32x16_bf16 v[136:151], v[172:175], v[98:101], v[136:151]\n"
        "v_rcp_f32 v158, v158\n"
        "v_rcp_f32 v159, v159\n"
        "v_rcp_f32 v160, v160\n"
        "v_rcp_f32 v161, v161\n"
        "v_rcp_f32 v162, v162\n"
        "v_rcp_f32 v163, v163\n"
        "v_rcp_f32 v164, v164\n"
        "v_rcp_f32 v165, v165\n"
        "v_rcp_f32 v166, v166\n"
        "v_rcp_f32 v167, v167\n"
        "v_pk_mul_f32 v[152:153], v[152:153], v[120:121]\n"
        "v_pk_mul_f32 v[154:155], v[154:155], v[122:123]\n"
        "v_pk_mul_f32 v[156:157], v[156:157], v[124:125]\n"
        "v_pk_mul_f32 v[158:159], v[158:159], v[126:127]\n"
        "v_pk_mul_f32 v[160:161], v[160:161], v[128:129]\n"
        "v_pk_mul_f32 v[162:163], v[162:163], v[130:131]\n"
        "v_pk_mul_f32 v[164:165], v[164:165], v[132:133]\n"
        "v_pk_mul_f32 v[166:167], v[166:167], v[134:135]\n"
        "v_cvt_pk_bf16_f32 v78, v152, v153\n"
        "v_cvt_pk_bf16_f32 v79, v154, v155\n"
        "v_cvt_pk_bf16_f32 v80, v156, v157\n"
        "v_cvt_pk_bf16_f32 v81, v158, v159\n"
        "v_cvt_pk_bf16_f32 v82, v160, v161\n"
        "v_cvt_pk_bf16_f32 v83, v162, v163\n"
        "v_cvt_pk_bf16_f32 v84, v164, v165\n"
        "v_cvt_pk_bf16_f32 v85, v166, v167\n"
        "v_mfma_f32_32x32x16_bf16 v[46:61], v[78:81], v[86:89], v[46:61]\n"
        "s_waitcnt vmcnt(0)\n"
        "v_exp_f32 v152, v136\n"
        "v_exp_f32 v153, v137\n"
        "v_exp_f32 v154, v138\n"
        "v_mfma_f32_32x32x16_bf16 v[46:61], v[82:85], v[90:93], v[46:61]\n"
        "v_exp_f32 v155, v139\n"
        "v_exp_f32 v156, v140\n"
        "v_exp_f32 v157, v141\n"
        "v_exp_f32 v158, v142\n"
        "v_exp_f32 v159, v143\n"
        "v_exp_f32 v160, v144\n"
        "v_exp_f32 v161, v145\n"
        "s_mov_b32 m0, s95\n"
        "ds_read_b128 v[34:37], v21 offset:0\n"
        "ds_read_b128 v[168:171], v30 offset:0\n"
        "ds_read_b128 v[172:175], v31 offset:2048\n"
        "buffer_load_dwordx4 v[38:39], s[68:71], 0 idxen offen offset:0 sc1 lds\n"
        "buffer_load_dwordx4 v[40:41], s[68:71], 0 idxen offen offset:1024 sc1 lds\n"
        "buffer_load_dwordx4 v[42:43], s[68:71], 0 idxen offen offset:2048 sc1 lds\n"
        "buffer_load_dwordx4 v[44:45], s[68:71], 0 idxen offen offset:3072 sc1 lds\n"
        "s_sub_u32 s92, s92, 1\n"
        "s_cmp_gt_u32 s92, 2\n"
        "s_cbranch_scc1 L_p31%=\n"
        "s_setprio 2\n"
        "L_p31%=:\n"
        "v_exp_f32 v162, v146\n"
        "v_exp_f32 v163, v147\n"
        "v_exp_f32 v164, v148\n"
        "v_exp_f32 v165, v149\n"
        "v_exp_f32 v166, v150\n"
        "v_exp_f32 v167, v151\n"
        "v_pk_fma_f32 v[152:153], v[152:153], s[20:21], s[20:21]\n"
        "v_pk_fma_f32 v[154:155], v[154:155], s[20:21], s[20:21]\n"
        "v_pk_fma_f32 v[156:157], v[156:157], s[20:21], s[20:21]\n"
        "v_pk_fma_f32 v[158:159], v[158:159], s[20:21], s[20:21]\n"
        "v_pk_fma_f32 v[160:161], v[160:161], s[20:21], s[20:21]\n"
        "v_pk_fma_f32 v[162:163], v[162:163], s[20:21], s[20:21]\n"
        "v_pk_fma_f32 v[164:165], v[164:165], s[20:21], s[20:21]\n"
        "v_pk_fma_f32 v[166:167], v[166:167], s[20:21], s[20:21]\n"
        "s_waitcnt lgkmcnt(0)\n"
        "v_mfma_f32_32x32x16_bf16 v[120:135], v[34:37], v[22:25], 0\n"
        "v_rcp_f32 v152, v152\n"
        "v_rcp_f32 v153, v153\n"
        "v_rcp_f32 v154, v154\n"
        "v_mfma_f32_32x32x16_bf16 v[120:135], v[168:171], v[26:29], v[120:135]\n"
        "v_rcp_f32 v155, v155\n"
        "v_rcp_f32 v156, v156\n"
        "v_rcp_f32 v157, v157\n"
        "v_mfma_f32_32x32x16_bf16 v[120:135], v[172:175], v[94:97], v[120:135]\n"
        "v_rcp_f32 v158, v158\n"
        "v_add_u32 v31, 2048, v31\n"
        "v_rcp_f32 v159, v159\n"
        "v_add_u32 v32, 2048, v32\n"
        "v_rcp_f32 v160, v160\n"
        "v_add_u32 v33, 256, v33\n"
        "v_rcp_f32 v161, v161\n"
        "v_rcp_f32 v162, v162\n"
        "v_rcp_f32 v163, v163\n"
        "v_rcp_f32 v164, v164\n"
        "v_rcp_f32 v165, v165\n"
        "v_rcp_f32 v166, v166\n"
        "v_rcp_f32 v167, v167\n"
        "v_pk_mul_f32 v[152:153], v[152:153], v[136:137]\n"
        "v_pk_mul_f32 v[154:155], v[154:155], v[138:139]\n"
        "v_pk_mul_f32 v[156:157], v[156:157], v[140:141]\n"
        "v_pk_mul_f32 v[158:159], v[158:159], v[142:143]\n"
        "v_pk_mul_f32 v[160:161], v[160:161], v[144:145]\n"
        "v_pk_mul_f32 v[162:163], v[162:163], v[146:147]\n"
        "v_pk_mul_f32 v[164:165], v[164:165], v[148:149]\n"
        "v_pk_mul_f32 v[166:167], v[166:167], v[150:151]\n"
        "v_cvt_pk_bf16_f32 v78, v152, v153\n"
        "v_cvt_pk_bf16_f32 v79, v154, v155\n"
        "v_cvt_pk_bf16_f32 v80, v156, v157\n"
        "v_cvt_pk_bf16_f32 v81, v158, v159\n"
        "v_cvt_pk_bf16_f32 v82, v160, v161\n"
        "v_cvt_pk_bf16_f32 v83, v162, v163\n"
        "v_cvt_pk_bf16_f32 v84, v164, v165\n"
        "v_cvt_pk_bf16_f32 v85, v166, v167\n"
        "s_cmp_gt_u32 s92, 1\n"
        "s_cbranch_scc1 L_loop%=\n"
        "s_branch L_tail%=\n"
        "L_odd%=:\n"
        "v_add_u32 v21, 0x8000, v21\n"
        "v_add_u32 v30, 0x8000, v30\n"
        "v_add_u32 v32, 1024, v32\n"
        "L_tail%=:\n"
        "s_setprio 1\n"
        "v_mov_b32 v2, 0\n"
        "ds_read_b128 v[34:37], v21 offset:64\n"
        "ds_read_b128 v[168:171], v30 offset:64\n"
        "ds_read_b32 v3, v2 offset:55328\n"
        "s_add_u32 s18, s12, 65536\n"
        "s_mov_b32 m0, s18\n"
        "s_sub_u32 s19, s12, 32768\n"
        "s_cmp_eq_u32 s33, 0\n"
        "s_cbranch_scc1 L_t1%=\n"
        "s_cmp_eq_u32 s33, 1\n"
        "s_cbranch_scc1 L_t2%=\n"
        "s_cmp_eq_u32 s33, 2\n"
        "s_cbranch_scc1 L_t3%=\n"
        "v_mfma_f32_32x32x16_bf16 v[62:77], v[78:81], v[86:89], v[62:77]\n"
        "v_exp_f32 v152, v120\n"
        "v_exp_f32 v153, v121\n"
        "v_exp_f32 v154, v122\n"
        "v_mfma_f32_32x32x16_bf16 v[62:77], v[82:85], v[90:93], v[62:77]\n"
        "v_exp_f32 v155, v123\n"
        "v_exp_f32 v156, v124\n"
        "v_exp_f32 v157, v125\n"
        "ds_read_b64_tr_b16 v[86:87], v32 offset:0\n"
        "ds_read_b64_tr_b16 v[88:89], v32 offset:256\n"
        "ds_read_b64_tr_b16 v[90:91], v32 offset:512\n"
        "ds_read_b64_tr_b16 v[92:93], v32 offset:768\n"
        "s_waitcnt lgkmcnt(4)\n"
        "v_mfma_f32_32x32x16_bf16 v[136:151], v[34:37], v[22:25], 0\n"
        "v_readfirstlane_b32 s18, v3\n"
        "v_exp_f32 v158, v126\n"
        "v_exp_f32 v159, v127\n"
        "s_cmp_ge_u32 s18, 8\n"
        "s_cbranch_scc1 L_cuok4%=\n"
        "L_cu4%=:\n"
        "ds_read_b32 v3, v2 offset:55328\n"
        "s_waitcnt lgkmcnt(0)\n"
        "v_readfirstlane_b32 s18, v3\n"
        "s_cmp_lt_u32 s18, 8\n"
        "s_cbranch_scc1 L_cu4%=\n"
        "L_cuok4%=:\n"
        "v_mfma_f32_32x32x16_bf16 v[136:151], v[168:171], v[26:29], v[136:151]\n"
        "v_exp_f32 v160, v128\n"
        "v_exp_f32 v161, v129\n"
        "v_exp_f32 v162, v130\n"
        "v_mfma_f32_32x32x16_bf16 v[136:151], v[172:175], v[98:101], v[136:151]\n"
        "v_exp_f32 v163, v131\n"
        "v_exp_f32 v164, v132\n"
        "v_exp_f32 v165, v133\n"
        "v_exp_f32 v166, v134\n"
        "v_exp_f32 v167, v135\n"
        "buffer_load_dwordx4 v8, s[56:59], s19 offen offset:0 lds\n"
        "buffer_load_dwordx4 v8, s[56:59], s19 offen offset:1024 lds\n"
        "buffer_load_dwordx4 v8, s[56:59], s19 offen offset:2048 lds\n"
        "buffer_load_dwordx4 v8, s[56:59], s19 offen offset:3072 lds\n"
        "v_pk_fma_f32 v[152:153], v[152:153], s[20:21], s[20:21]\n"
        "v_pk_fma_f32 v[154:155], v[154:155], s[20:21], s[20:21]\n"
        "v_pk_fma_f32 v[156:157], v[156:157], s[20:21], s[20:21]\n"
        "v_pk_fma_f32 v[158:159], v[158:159], s[20:21], s[20:21]\n"
        "v_pk_fma_f32 v[160:161], v[160:161], s[20:21], s[20:21]\n"
        "v_pk_fma_f32 v[162:163], v[162:163], s[20:21], s[20:21]\n"
        "v_pk_fma_f32 v[164:165], v[164:165], s[20:21], s[20:21]\n"
        "v_pk_fma_f32 v[166:167], v[166:167], s[20:21], s[20:21]\n"
        "v_exp_f32 v176, v136\n"
        "v_exp_f32 v177, v137\n"
        "v_exp_f32 v178, v138\n"
        "v_exp_f32 v179, v139\n"
        "v_exp_f32 v180, v140\n"
        "v_exp_f32 v181, v141\n"
        "v_exp_f32 v182, v142\n"
        "v_exp_f32 v183, v143\n"
        "v_exp_f32 v184, v144\n"
        "v_exp_f32 v185, v145\n"
        "v_exp_f32 v186, v146\n"
        "v_exp_f32 v187, v147\n"
        "v_exp_f32 v188, v148\n"
        "v_exp_f32 v189, v149\n"
        "v_exp_f32 v190, v150\n"
        "v_exp_f32 v191, v151\n"
        "v_rcp_f32 v152, v152\n"
        "v_rcp_f32 v153, v153\n"
        "v_rcp_f32 v154, v154\n"
        "v_rcp_f32 v155, v155\n"
        "v_rcp_f32 v156, v156\n"
        "v_rcp_f32 v157, v157\n"
        "v_rcp_f32 v158, v158\n"
        "v_rcp_f32 v159, v159\n"
        "v_rcp_f32 v160, v160\n"
        "v_rcp_f32 v161, v161\n"
        "v_rcp_f32 v162, v162\n"
        "v_rcp_f32 v163, v163\n"
        "v_rcp_f32 v164, v164\n"
        "v_rcp_f32 v165, v165\n"
        "v_rcp_f32 v166, v166\n"
        "v_rcp_f32 v167, v167\n"
        "v_pk_fma_f32 v[176:177], v[176:177], s[20:21], s[20:21]\n"
        "v_pk_fma_f32 v[178:179], v[178:179], s[20:21], s[20:21]\n"
        "v_pk_fma_f32 v[180:181], v[180:181], s[20:21], s[20:21]\n"
        "v_pk_fma_f32 v[182:183], v[182:183], s[20:21], s[20:21]\n"
        "v_pk_fma_f32 v[184:185], v[184:185], s[20:21], s[20:21]\n"
        "v_pk_fma_f32 v[186:187], v[186:187], s[20:21], s[20:21]\n"
        "v_pk_fma_f32 v[188:189], v[188:189], s[20:21], s[20:21]\n"
        "v_pk_fma_f32 v[190:191], v[190:191], s[20:21], s[20:21]\n"
        "v_pk_mul_f32 v[152:153], v[152:153], v[120:121]\n"
        "v_pk_mul_f32 v[154:155], v[154:155], v[122:123]\n"
        "v_pk_mul_f32 v[156:157], v[156:157], v[124:125]\n"
        "v_pk_mul_f32 v[158:159], v[158:159], v[126:127]\n"
        "v_pk_mul_f32 v[160:161], v[160:161], v[128:129]\n"
        "v_pk_mul_f32 v[162:163], v[162:163], v[130:131]\n"
        "v_pk_mul_f32 v[164:165], v[164:165], v[132:133]\n"
        "v_pk_mul_f32 v[166:167], v[166:167], v[134:135]\n"
        "v_cvt_pk_bf16_f32 v78, v152, v153\n"
        "v_cvt_pk_bf16_f32 v79, v154, v155\n"
        "v_cvt_pk_bf16_f32 v80, v156, v157\n"
        "v_cvt_pk_bf16_f32 v81, v158, v159\n"
        "v_cvt_pk_bf16_f32 v82, v160, v161\n"
        "v_cvt_pk_bf16_f32 v83, v162, v163\n"
        "v_cvt_pk_bf16_f32 v84, v164, v165\n"
        "v_cvt_pk_bf16_f32 v85, v166, v167\n"
        "s_setprio 0\n"
        "v_rcp_f32 v176, v176\n"
        "s_waitcnt lgkmcnt(0)\n"
        "v_mfma_f32_32x32x16_bf16 v[46:61], v[78:81], v[86:89], v[46:61]\n"
        "v_rcp_f32 v177, v177\n"
        "v_rcp_f32 v178, v178\n"
        "v_rcp_f32 v179, v179\n"
        "v_mfma_f32_32x32x16_bf16 v[46:61], v[82:85], v[90:93], v[46:61]\n"
        "v_rcp_f32 v180, v180\n"
        "v_rcp_f32 v181, v181\n"
        "v_rcp_f32 v182, v182\n"
        "v_rcp_f32 v183, v183\n"
        "v_rcp_f32 v184, v184\n"
        "v_rcp_f32 v185, v185\n"
        "v_rcp_f32 v186, v186\n"
        "v_rcp_f32 v187, v187\n"
        "v_rcp_f32 v188, v188\n"
        "v_rcp_f32 v189, v189\n"
        "v_rcp_f32 v190, v190\n"
        "v_rcp_f32 v191, v191\n"
        "v_pk_mul_f32 v[176:177], v[176:177], v[136:137]\n"
        "v_pk_mul_f32 v[178:179], v[178:179], v[138:139]\n"
        "v_pk_mul_f32 v[180:181], v[180:181], v[140:141]\n"
        "v_pk_mul_f32 v[182:183], v[182:183], v[142:143]\n"
        "v_pk_mul_f32 v[184:185], v[184:185], v[144:145]\n"
        "v_pk_mul_f32 v[186:187], v[186:187], v[146:147]\n"
        "v_pk_mul_f32 v[188:189], v[188:189], v[148:149]\n"
        "v_pk_mul_f32 v[190:191], v[190:191], v[150:151]\n"
        "v_cvt_pk_bf16_f32 v192, v176, v177\n"
        "v_cvt_pk_bf16_f32 v193, v178, v179\n"
        "v_cvt_pk_bf16_f32 v194, v180, v181\n"
        "v_cvt_pk_bf16_f32 v195, v182, v183\n"
        "v_cvt_pk_bf16_f32 v196, v184, v185\n"
        "v_cvt_pk_bf16_f32 v197, v186, v187\n"
        "v_cvt_pk_bf16_f32 v198, v188, v189\n"
        "v_cvt_pk_bf16_f32 v199, v190, v191\n"
        "v_mfma_f32_32x32x16_bf16 v[62:77], v[192:195], v[86:89], v[62:77]\n"
        "s_nop 0\n"
        "v_mfma_f32_32x32x16_bf16 v[62:77], v[196:199], v[90:93], v[62:77]\n"
        "s_branch L_epi%=\n"
        "L_t3%=:\n"
        "v_mov_b32 v198, 0\n"
        "v_mov_b32 v199, 0\n"
        "v_mfma_f32_32x32x16_bf16 v[62:77], v[78:81], v[86:89], v[62:77]\n"
        "v_exp_f32 v152, v120\n"
        "v_exp_f32 v153, v121\n"
        "v_exp_f32 v154, v122\n"
        "v_mfma_f32_32x32x16_bf16 v[62:77], v[82:85], v[90:93], v[62:77]\n"
        "v_exp_f32 v155, v123\n"
        "v_exp_f32 v156, v124\n"
        "v_exp_f32 v157, v125\n"
        "ds_read_b64_tr_b16 v[86:87], v32 offset:0\n"
        "ds_read_b64_tr_b16 v[88:89], v32 offset:256\n"
        "ds_read_b64_tr_b16 v[90:91], v32 offset:512\n"
        "ds_read_b64_tr_b16 v[92:93], v32 offset:768\n"
        "s_waitcnt lgkmcnt(4)\n"
        "v_mfma_f32_32x32x16_bf16 v[136:151], v[34:37], v[22:25], 0\n"
        "v_readfirstlane_b32 s18, v3\n"
        "v_exp_f32 v158, v126\n"
        "v_exp_f32 v159, v127\n"
        "s_cmp_ge_u32 s18, 8\n"
        "s_cbranch_scc1 L_cuok3%=\n"
        "L_cu3%=:\n"
        "ds_read_b32 v3, v2 offset:55328\n"
        "s_waitcnt lgkmcnt(0)\n"
        "v_readfirstlane_b32 s18, v3\n"
        "s_cmp_lt_u32 s18, 8\n"
        "s_cbranch_scc1 L_cu3%=\n"
        "L_cuok3%=:\n"
        "v_mfma_f32_32x32x16_bf16 v[136:151], v[168:171], v[26:29], v[136:151]\n"
        "v_exp_f32 v160, v128\n"
        "v_exp_f32 v161, v129\n"
        "v_exp_f32 v162, v130\n"
        "v_mfma_f32_32x32x16_bf16 v[136:151], v[172:175], v[98:101], v[136:151]\n"
        "v_exp_f32 v163, v131\n"
        "buffer_load_dwordx4 v8, s[56:59], s19 offen offset:0 lds\n"
        "buffer_load_dwordx4 v8, s[56:59], s19 offen offset:1024 lds\n"
        "buffer_load_dwordx4 v8, s[56:59], s19 offen offset:2048 lds\n"
        "buffer_load_dwordx4 v8, s[56:59], s19 offen offset:3072 lds\n"
        "v_pk_fma_f32 v[152:153], v[152:153], s[20:21], s[20:21]\n"
        "v_pk_fma_f32 v[154:155], v[154:155], s[20:21], s[20:21]\n"
        "v_pk_fma_f32 v[156:157], v[156:157], s[20:21], s[20:21]\n"
        "v_pk_fma_f32 v[158:159], v[158:159], s[20:21], s[20:21]\n"
        "v_pk_fma_f32 v[160:161], v[160:161], s[20:21], s[20:21]\n"
        "v_pk_fma_f32 v[162:163], v[162:163], s[20:21], s[20:21]\n"
        "s_nop 0\n"
        "v_exp_f32 v176, v136\n"
        "v_exp_f32 v177, v137\n"
        "v_exp_f32 v178, v138\n"
        "v_exp_f32 v179, v139\n"
        "v_exp_f32 v180, v140\n"
        "v_exp_f32 v181, v141\n"
        "v_exp_f32 v182, v142\n"
        "v_exp_f32 v183, v143\n"
        "v_exp_f32 v184, v144\n"
        "v_exp_f32 v185, v145\n"
        "v_exp_f32 v186, v146\n"
        "v_exp_f32 v187, v147\n"
        "v_rcp_f32 v152, v152\n"
        "v_rcp_f32 v153, v153\n"
        "v_rcp_f32 v154, v154\n"
        "v_rcp_f32 v155, v155\n"
        "v_rcp_f32 v156, v156\n"
        "v_rcp_f32 v157, v157\n"
        "v_rcp_f32 v158, v158\n"
        "v_rcp_f32 v159, v159\n"
        "v_rcp_f32 v160, v160\n"
        "v_rcp_f32 v161, v161\n"
        "v_rcp_f32 v162, v162\n"
        "v_rcp_f32 v163, v163\n"
        "v_pk_fma_f32 v[176:177], v[176:177], s[20:21], s[20:21]\n"
        "v_pk_fma_f32 v[178:179], v[178:179], s[20:21], s[20:21]\n"
        "v_pk_fma_f32 v[180:181], v[180:181], s[20:21], s[20:21]\n"
        "v_pk_fma_f32 v[182:183], v[182:183], s[20:21], s[20:21]\n"
        "v_pk_fma_f32 v[184:185], v[184:185], s[20:21], s[20:21]\n"
        "v_pk_fma_f32 v[186:187], v[186:187], s[20:21], s[20:21]\n"
        "v_pk_mul_f32 v[152:153], v[152:153], v[120:121]\n"
        "v_pk_mul_f32 v[154:155], v[154:155], v[122:123]\n"
        "v_pk_mul_f32 v[156:157], v[156:157], v[124:125]\n"
        "v_pk_mul_f32 v[158:159], v[158:159], v[126:127]\n"
        "v_pk_mul_f32 v[160:161], v[160:161], v[128:129]\n"
        "v_pk_mul_f32 v[162:163], v[162:163], v[130:131]\n"
        "v_cvt_pk_bf16_f32 v78, v152, v153\n"
        "v_cvt_pk_bf16_f32 v79, v154, v155\n"
        "v_cvt_pk_bf16_f32 v80, v156, v157\n"
        "v_cvt_pk_bf16_f32 v81, v158, v159\n"
        "v_cvt_pk_bf16_f32 v82, v160, v161\n"
        "v_cvt_pk_bf16_f32 v83, v162, v163\n"
        "s_setprio 0\n"
        "v_rcp_f32 v176, v176\n"
        "s_waitcnt lgkmcnt(0)\n"
        "v_mfma_f32_32x32x16_bf16 v[46:61], v[78:81], v[86:89], v[46:61]\n"
        "v_rcp_f32 v177, v177\n"
        "v_rcp_f32 v178, v178\n"
        "v_rcp_f32 v179, v179\n"
        "v_mfma_f32_32x32x16_bf16 v[46:61], v[82:85], v[90:93], v[46:61]\n"
        "v_rcp_f32 v180, v180\n"
        "v_rcp_f32 v181, v181\n"
        "v_rcp_f32 v182, v182\n"
        "v_rcp_f32 v183, v183\n"
        "v_rcp_f32 v184, v184\n"
        "v_rcp_f32 v185, v185\n"
        "v_rcp_f32 v186, v186\n"
        "v_rcp_f32 v187, v187\n"
        "v_pk_mul_f32 v[176:177], v[176:177], v[136:137]\n"
        "v_pk_mul_f32 v[178:179], v[178:179], v[138:139]\n"
        "v_pk_mul_f32 v[180:181], v[180:181], v[140:141]\n"
        "v_pk_mul_f32 v[182:183], v[182:183], v[142:143]\n"
        "v_pk_mul_f32 v[184:185], v[184:185], v[144:145]\n"
        "v_pk_mul_f32 v[186:187], v[186:187], v[146:147]\n"
        "v_cvt_pk_bf16_f32 v192, v176, v177\n"
        "v_cvt_pk_bf16_f32 v193, v178, v179\n"
        "v_cvt_pk_bf16_f32 v194, v180, v181\n"
        "v_cvt_pk_bf16_f32 v195, v182, v183\n"
        "v_cvt_pk_bf16_f32 v196, v184, v185\n"
        "v_cvt_pk_bf16_f32 v197, v186, v187\n"
        "v_mfma_f32_32x32x16_bf16 v[62:77], v[192:195], v[86:89], v[62:77]\n"
        "s_nop 0\n"
        "v_mfma_f32_32x32x16_bf16 v[62:77], v[196:199], v[90:93], v[62:77]\n"
        "s_branch L_epi%=\n"
        "L_t2%=:\n"
        "v_mfma_f32_32x32x16_bf16 v[62:77], v[78:81], v[86:89], v[62:77]\n"
        "v_exp_f32 v152, v120\n"
        "v_exp_f32 v153, v121\n"
        "v_exp_f32 v154, v122\n"
        "v_mfma_f32_32x32x16_bf16 v[62:77], v[82:85], v[90:93], v[62:77]\n"
        "v_exp_f32 v155, v123\n"
        "v_exp_f32 v156, v124\n"
        "v_exp_f32 v157, v125\n"
        "ds_read_b64_tr_b16 v[86:87], v32 offset:0\n"
        "ds_read_b64_tr_b16 v[88:89], v32 offset:256\n"
        "ds_read_b64_tr_b16 v[90:91], v32 offset:512\n"
        "ds_read_b64_tr_b16 v[92:93], v32 offset:768\n"
        "s_waitcnt lgkmcnt(4)\n"
        "v_mfma_f32_32x32x16_bf16 v[136:151], v[34:37], v[22:25], 0\n"
        "v_readfirstlane_b32 s18, v3\n"
        "v_exp_f32 v158, v126\n"
        "v_exp_f32 v159, v127\n"
        "s_cmp_ge_u32 s18, 8\n"
        "s_cbranch_scc1 L_cuok2%=\n"
        "L_cu2%=:\n"
        "ds_read_b32 v3, v2 offset:55328\n"
        "s_waitcnt lgkmcnt(0)\n"
        "v_readfirstlane_b32 s18, v3\n"
        "s_cmp_lt_u32 s18, 8\n"
        "s_cbranch_scc1 L_cu2%=\n"
        "L_cuok2%=:\n"
        "v_mfma_f32_32x32x16_bf16 v[136:151], v[168:171], v[26:29], v[136:151]\n"
        "v_mfma_f32_32x32x16_bf16 v[136:151], v[172:175], v[98:101], v[136:151]\n"
        "buffer_load_dwordx4 v8, s[56:59], s19 offen offset:0 lds\n"
        "buffer_load_dwordx4 v8, s[56:59], s19 offen offset:1024 lds\n"
        "buffer_load_dwordx4 v8, s[56:59], s19 offen offset:2048 lds\n"
        "buffer_load_dwordx4 v8, s[56:59], s19 offen offset:3072 lds\n"
        "v_pk_fma_f32 v[152:153], v[152:153], s[20:21], s[20:21]\n"
        "v_pk_fma_f32 v[154:155], v[154:155], s[20:21], s[20:21]\n"
        "v_pk_fma_f32 v[156:157], v[156:157], s[20:21], s[20:21]\n"
        "v_pk_fma_f32 v[158:159], v[158:159], s[20:21], s[20:21]\n"
        "s_nop 3\n"
        "v_exp_f32 v176, v136\n"
        "v_exp_f32 v177, v137\n"
        "v_exp_f32 v178, v138\n"
        "v_exp_f32 v179, v139\n"
        "v_exp_f32 v180, v140\n"
        "v_exp_f32 v181, v141\n"
        "v_exp_f32 v182, v142\n"
        "v_exp_f32 v183, v143\n"
        "v_rcp_f32 v152, v152\n"
        "v_rcp_f32 v153, v153\n"
        "v_rcp_f32 v154, v154\n"
        "v_rcp_f32 v155, v155\n"
        "v_rcp_f32 v156, v156\n"
        "v_rcp_f32 v157, v157\n"
        "v_rcp_f32 v158, v158\n"
        "v_rcp_f32 v159, v159\n"
        "v_pk_fma_f32 v[176:177], v[176:177], s[20:21], s[20:21]\n"
        "v_pk_fma_f32 v[178:179], v[178:179], s[20:21], s[20:21]\n"
        "v_pk_fma_f32 v[180:181], v[180:181], s[20:21], s[20:21]\n"
        "v_pk_fma_f32 v[182:183], v[182:183], s[20:21], s[20:21]\n"
        "v_pk_mul_f32 v[152:153], v[152:153], v[120:121]\n"
        "v_pk_mul_f32 v[154:155], v[154:155], v[122:123]\n"
        "v_pk_mul_f32 v[156:157], v[156:157], v[124:125]\n"
        "v_pk_mul_f32 v[158:159], v[158:159], v[126:127]\n"
        "v_cvt_pk_bf16_f32 v78, v152, v153\n"
        "v_cvt_pk_bf16_f32 v79, v154, v155\n"
        "v_cvt_pk_bf16_f32 v80, v156, v157\n"
        "v_cvt_pk_bf16_f32 v81, v158, v159\n"
        "s_setprio 0\n"
        "v_rcp_f32 v176, v176\n"
        "s_waitcnt lgkmcnt(0)\n"
        "v_mfma_f32_32x32x16_bf16 v[46:61], v[78:81], v[86:89], v[46:61]\n"
        "v_rcp_f32 v177, v177\n"
        "v_rcp_f32 v178, v178\n"
        "v_rcp_f32 v179, v179\n"
        "v_rcp_f32 v180, v180\n"
        "v_rcp_f32 v181, v181\n"
        "v_rcp_f32 v182, v182\n"
        "v_rcp_f32 v183, v183\n"
        "v_pk_mul_f32 v[176:177], v[176:177], v[136:137]\n"
        "v_pk_mul_f32 v[178:179], v[178:179], v[138:139]\n"
        "v_pk_mul_f32 v[180:181], v[180:181], v[140:141]\n"
        "v_pk_mul_f32 v[182:183], v[182:183], v[142:143]\n"
        "v_cvt_pk_bf16_f32 v192, v176, v177\n"
        "v_cvt_pk_bf16_f32 v193, v178, v179\n"
        "v_cvt_pk_bf16_f32 v194, v180, v181\n"
        "v_cvt_pk_bf16_f32 v195, v182, v183\n"
        "s_nop 1\n"
        "v_mfma_f32_32x32x16_bf16 v[62:77], v[192:195], v[86:89], v[62:77]\n"
        "s_branch L_epi%=\n"
        "L_t1%=:\n"
        "v_mov_b32 v194, 0\n"
        "v_mov_b32 v195, 0\n"
        "v_mfma_f32_32x32x16_bf16 v[62:77], v[78:81], v[86:89], v[62:77]\n"
        "v_exp_f32 v152, v120\n"
        "v_exp_f32 v153, v121\n"
        "v_exp_f32 v154, v122\n"
        "v_mfma_f32_32x32x16_bf16 v[62:77], v[82:85], v[90:93], v[62:77]\n"
        "v_exp_f32 v155, v123\n"
        "ds_read_b64_tr_b16 v[86:87], v32 offset:0\n"
        "ds_read_b64_tr_b16 v[88:89], v32 offset:256\n"
        "ds_read_b64_tr_b16 v[90:91], v32 offset:512\n"
        "ds_read_b64_tr_b16 v[92:93], v32 offset:768\n"
        "s_waitcnt lgkmcnt(4)\n"
        "v_mfma_f32_32x32x16_bf16 v[136:151], v[34:37], v[22:25], 0\n"
        "v_readfirstlane_b32 s18, v3\n"
        "s_cmp_ge_u32 s18, 8\n"
        "s_cbranch_scc1 L_cuok1%=\n"
        "L_cu1%=:\n"
        "ds_read_b32 v3, v2 offset:55328\n"
        "s_waitcnt lgkmcnt(0)\n"
        "v_readfirstlane_b32 s18, v3\n"
        "s_cmp_lt_u32 s18, 8\n"
        "s_cbranch_scc1 L_cu1%=\n"
        "L_cuok1%=:\n"
        "v_mfma_f32_32x32x16_bf16 v[136:151], v[168:171], v[26:29], v[136:151]\n"
        "v_mfma_f32_32x32x16_bf16 v[136:151], v[172:175], v[98:101], v[136:151]\n"
        "buffer_load_dwordx4 v8, s[56:59], s19 offen offset:0 lds\n"
        "buffer_load_dwordx4 v8, s[56:59], s19 offen offset:1024 lds\n"
        "buffer_load_dwordx4 v8, s[56:59], s19 offen offset:2048 lds\n"
        "buffer_load_dwordx4 v8, s[56:59], s19 offen offset:3072 lds\n"
        "v_pk_fma_f32 v[152:153], v[152:153], s[20:21], s[20:21]\n"
        "v_pk_fma_f32 v[154:155], v[154:155], s[20:21], s[20:21]\n"
        "s_nop 5\n"
        "v_exp_f32 v176, v136\n"
        "v_exp_f32 v177, v137\n"
        "v_exp_f32 v178, v138\n"
        "v_exp_f32 v179, v139\n"
        "v_rcp_f32 v152, v152\n"
        "v_rcp_f32 v153, v153\n"
        "v_rcp_f32 v154, v154\n"
        "v_rcp_f32 v155, v155\n"
        "v_pk_fma_f32 v[176:177], v[176:177], s[20:21], s[20:21]\n"
        "v_pk_fma_f32 v[178:179], v[178:179], s[20:21], s[20:21]\n"
        "v_pk_mul_f32 v[152:153], v[152:153], v[120:121]\n"
        "v_pk_mul_f32 v[154:155], v[154:155], v[122:123]\n"
        "v_cvt_pk_bf16_f32 v78, v152, v153\n"
        "v_cvt_pk_bf16_f32 v79, v154, v155\n"
        "s_setprio 0\n"
        "v_rcp_f32 v176, v176\n"
        "s_waitcnt lgkmcnt(0)\n"
        "v_mfma_f32_32x32x16_bf16 v[46:61], v[78:81], v[86:89], v[46:61]\n"
        "v_rcp_f32 v177, v177\n"
        "v_rcp_f32 v178, v178\n"
        "v_rcp_f32 v179, v179\n"
        "v_pk_mul_f32 v[176:177], v[176:177], v[136:137]\n"
        "v_pk_mul_f32 v[178:179], v[178:179], v[138:139]\n"
        "v_cvt_pk_bf16_f32 v192, v176, v177\n"
        "v_cvt_pk_bf16_f32 v193, v178, v179\n"
        "s_nop 1\n"
        "v_mfma_f32_32x32x16_bf16 v[62:77], v[192:195], v[86:89], v[62:77]\n"
        "L_epi%=:\n"
        "s_lshl_b32 s16, s15, 12\n"
        "s_lshl_b32 s17, s90, 4\n"
        "s_add_u32 s16, s16, s17\n"
        "s_add_u32 s16, s16, 32768\n"
        "v_cmp_lt_u32 vcc, 7, v9\n"
        "v_lshl_add_u32 v2, v9, 6, s16\n"
        "v_lshl_add_u32 v2, v10, 9, v2\n"
        "v_add_u32 v3, 7680, v2\n"
        "v_cndmask_b32 v2, v2, v3, vcc\n"
        "s_lshl_b32 s17, s14, 13\n"
        "s_add_u32 s17, s17, 32768\n"
        "v_lshl_add_u32 v3, v1, 4, s17\n"
        "v_lshl_add_u32 v4, v10, 10, v14\n"
        "v_add_u32 v5, s12, v19\n"
        "v_xor_b32 v6, 16, v5\n"
        "v_add_u32 v7, s12, v20\n"
        "s_mov_b32 s28, 0x3ff\n"
        "s_mov_b32 s29, 0x3ff\n"
        "v_cvt_pk_bf16_f32 v152, v46, v47\n"
        "v_cvt_pk_bf16_f32 v153, v48, v49\n"
        "v_cvt_pk_bf16_f32 v154, v50, v51\n"
        "v_cvt_pk_bf16_f32 v155, v52, v53\n"
        "v_cvt_pk_bf16_f32 v156, v54, v55\n"
        "v_cvt_pk_bf16_f32 v157, v56, v57\n"
        "v_cvt_pk_bf16_f32 v158, v58, v59\n"
        "v_cvt_pk_bf16_f32 v159, v60, v61\n"
        "s_mov_b64 exec, s[28:29]\n"
        "ds_write_b128 v2, v[152:155] offset:0\n"
        "ds_write_b128 v2, v[156:159] offset:1024\n"
        "s_mov_b64 exec, -1\n"
        "v_cvt_pk_bf16_f32 v160, v62, v63\n"
        "v_cvt_pk_bf16_f32 v161, v64, v65\n"
        "v_cvt_pk_bf16_f32 v162, v66, v67\n"
        "v_cvt_pk_bf16_f32 v163, v68, v69\n"
        "v_cvt_pk_bf16_f32 v164, v70, v71\n"
        "v_cvt_pk_bf16_f32 v165, v72, v73\n"
        "v_cvt_pk_bf16_f32 v166, v74, v75\n"
        "v_cvt_pk_bf16_f32 v167, v76, v77\n"
        "s_mov_b64 exec, s[28:29]\n"
        "ds_write_b128 v2, v[160:163] offset:2048\n"
        "ds_write_b128 v2, v[164:167] offset:3072\n"
        "s_mov_b64 exec, -1\n"
        "s_waitcnt vmcnt(0)\n"
        "s_cmp_ge_u32 s8, 4\n"
        "s_cbranch_scc1 L_ep%=\n"
        "s_setprio 1\n"
        "L_ep%=:\n"
        "s_waitcnt lgkmcnt(0)\n"
        "s_barrier\n"
        "ds_read_b64_tr_b16 v[120:121], v4 offset:0\n"
        "ds_read_b64_tr_b16 v[122:123], v4 offset:2048\n"
        "ds_read_b128 v[168:171], v3 offset:0\n"
        "ds_read_b64_tr_b16 v[124:125], v4 offset:4096\n"
        "ds_read_b64_tr_b16 v[126:127], v4 offset:6144\n"
        "ds_read_b128 v[172:175], v3 offset:1024\n"
        "ds_read_b64_tr_b16 v[128:129], v4 offset:8192\n"
        "ds_read_b64_tr_b16 v[130:131], v4 offset:10240\n"
        "ds_read_b128 v[176:179], v3 offset:2048\n"
        "ds_read_b64_tr_b16 v[132:133], v4 offset:12288\n"
        "ds_read_b64_tr_b16 v[134:135], v4 offset:14336\n"
        "ds_read_b128 v[180:183], v3 offset:3072\n"
        "s_waitcnt lgkmcnt(9)\n"
        "v_mfma_f32_32x32x16_bf16 v[104:119], v[120:123], v[168:171], v[104:119]\n"
        "ds_read_b64_tr_b16 v[136:137], v4 offset:16384\n"
        "ds_read_b64_tr_b16 v[138:139], v4 offset:18432\n"
        "ds_read_b128 v[184:187], v3 offset:4096\n"
        "s_waitcnt lgkmcnt(9)\n"
        "v_mfma_f32_32x32x16_bf16 v[104:119], v[124:127], v[172:175], v[104:119]\n"
        "ds_read_b64_tr_b16 v[140:141], v4 offset:20480\n"
        "ds_read_b64_tr_b16 v[142:143], v4 offset:22528\n"
        "ds_read_b128 v[188:191], v3 offset:5120\n"
        "s_waitcnt lgkmcnt(9)\n"
        "v_mfma_f32_32x32x16_bf16 v[104:119], v[128:131], v[176:179], v[104:119]\n"
        "ds_read_b64_tr_b16 v[144:145], v4 offset:24576\n"
        "ds_read_b64_tr_b16 v[146:147], v4 offset:26624\n"
        "ds_read_b128 v[192:195], v3 offset:6144\n"
        "s_waitcnt lgkmcnt(9)\n"
        "v_mfma_f32_32x32x16_bf16 v[104:119], v[132:135], v[180:183], v[104:119]\n"
        "ds_read_b64_tr_b16 v[148:149], v4 offset:28672\n"
        "ds_read_b64_tr_b16 v[150:151], v4 offset:30720\n"
        "ds_read_b128 v[196:199], v3 offset:7168\n"
        "s_waitcnt lgkmcnt(9)\n"
        "v_mfma_f32_32x32x16_bf16 v[104:119], v[136:139], v[184:187], v[104:119]\n"
        "s_waitcnt lgkmcnt(6)\n"
        "v_mfma_f32_32x32x16_bf16 v[104:119], v[140:143], v[188:191], v[104:119]\n"
        "s_waitcnt lgkmcnt(3)\n"
        "v_mfma_f32_32x32x16_bf16 v[104:119], v[144:147], v[192:195], v[104:119]\n"
        "s_waitcnt lgkmcnt(0)\n"
        "v_mfma_f32_32x32x16_bf16 v[104:119], v[148:151], v[196:199], v[104:119]\n"
        "s_nop 8\n"
        "s_cmp_lt_u32 s8, 4\n"
        "s_cbranch_scc1 L_eg%=\n"
        "s_mov_b32 exec_lo, 0xff\n"
        "s_mov_b32 exec_hi, 0xff\n"
        "ds_write_b128 v5, v[104:107] offset:0\n"
        "ds_write_b128 v5, v[108:111] offset:16\n"
        "ds_write_b128 v5, v[112:115] offset:32\n"
        "ds_write_b128 v5, v[116:119] offset:48\n"
        "s_mov_b64 exec, -1\n"
        "ds_read_b128 v[46:49], v7\n"
        "s_waitcnt lgkmcnt(0)\n"
        "v_pk_mul_f32 v[152:153], v[46:47], s[20:21]\n"
        "v_pk_mul_f32 v[154:155], v[48:49], s[20:21]\n"
        "v_exp_f32 v152, v152\n"
        "v_exp_f32 v153, v153\n"
        "v_exp_f32 v154, v154\n"
        "v_exp_f32 v155, v155\n"
        "v_pk_add_f32 v[152:153], v[152:153], 1.0 op_sel_hi:[1,0]\n"
        "v_pk_add_f32 v[154:155], v[154:155], 1.0 op_sel_hi:[1,0]\n"
        "v_rcp_f32 v152, v152\n"
        "v_rcp_f32 v153, v153\n"
        "v_rcp_f32 v154, v154\n"
        "v_rcp_f32 v155, v155\n"
        "v_pk_mul_f32 v[152:153], v[152:153], v[46:47]\n"
        "v_pk_mul_f32 v[154:155], v[154:155], v[48:49]\n"
        "v_cvt_pk_bf16_f32 v62, v152, v153\n"
        "v_cvt_pk_bf16_f32 v63, v154, v155\n"
        "buffer_store_dwordx2 v[62:63], v18, s[60:63], 0 offen sc1 nt\n"
        "s_branch L_fin%=\n"
        "L_eg%=:\n"
        "v_add_u32 v2, 0x100000, v18\n"
        "v_pk_mul_f32 v[152:153], v[104:105], s[20:21]\n"
        "v_pk_mul_f32 v[154:155], v[106:107], s[20:21]\n"
        "v_pk_mul_f32 v[156:157], v[108:109], s[20:21]\n"
        "v_pk_mul_f32 v[158:159], v[110:111], s[20:21]\n"
        "v_pk_mul_f32 v[160:161], v[112:113], s[20:21]\n"
        "v_pk_mul_f32 v[162:163], v[114:115], s[20:21]\n"
        "v_pk_mul_f32 v[164:165], v[116:117], s[20:21]\n"
        "v_pk_mul_f32 v[166:167], v[118:119], s[20:21]\n"
        "v_exp_f32 v152, v152\n"
        "v_exp_f32 v153, v153\n"
        "v_exp_f32 v154, v154\n"
        "v_exp_f32 v155, v155\n"
        "v_exp_f32 v156, v156\n"
        "v_exp_f32 v157, v157\n"
        "v_exp_f32 v158, v158\n"
        "v_exp_f32 v159, v159\n"
        "v_exp_f32 v160, v160\n"
        "v_exp_f32 v161, v161\n"
        "v_exp_f32 v162, v162\n"
        "v_exp_f32 v163, v163\n"
        "v_exp_f32 v164, v164\n"
        "v_exp_f32 v165, v165\n"
        "v_exp_f32 v166, v166\n"
        "v_exp_f32 v167, v167\n"
        "v_pk_add_f32 v[152:153], v[152:153], 1.0 op_sel_hi:[1,0]\n"
        "v_pk_add_f32 v[154:155], v[154:155], 1.0 op_sel_hi:[1,0]\n"
        "v_pk_add_f32 v[156:157], v[156:157], 1.0 op_sel_hi:[1,0]\n"
        "v_pk_add_f32 v[158:159], v[158:159], 1.0 op_sel_hi:[1,0]\n"
        "v_pk_add_f32 v[160:161], v[160:161], 1.0 op_sel_hi:[1,0]\n"
        "v_pk_add_f32 v[162:163], v[162:163], 1.0 op_sel_hi:[1,0]\n"
        "v_pk_add_f32 v[164:165], v[164:165], 1.0 op_sel_hi:[1,0]\n"
        "v_pk_add_f32 v[166:167], v[166:167], 1.0 op_sel_hi:[1,0]\n"
        "v_rcp_f32 v152, v152\n"
        "v_rcp_f32 v153, v153\n"
        "v_rcp_f32 v154, v154\n"
        "v_rcp_f32 v155, v155\n"
        "v_rcp_f32 v156, v156\n"
        "v_rcp_f32 v157, v157\n"
        "v_rcp_f32 v158, v158\n"
        "v_rcp_f32 v159, v159\n"
        "v_rcp_f32 v160, v160\n"
        "v_rcp_f32 v161, v161\n"
        "v_rcp_f32 v162, v162\n"
        "v_rcp_f32 v163, v163\n"
        "v_rcp_f32 v164, v164\n"
        "v_rcp_f32 v165, v165\n"
        "v_rcp_f32 v166, v166\n"
        "v_rcp_f32 v167, v167\n"
        "v_pk_mul_f32 v[152:153], v[152:153], v[104:105]\n"
        "v_pk_mul_f32 v[154:155], v[154:155], v[106:107]\n"
        "v_pk_mul_f32 v[156:157], v[156:157], v[108:109]\n"
        "v_pk_mul_f32 v[158:159], v[158:159], v[110:111]\n"
        "v_pk_mul_f32 v[160:161], v[160:161], v[112:113]\n"
        "v_pk_mul_f32 v[162:163], v[162:163], v[114:115]\n"
        "v_pk_mul_f32 v[164:165], v[164:165], v[116:117]\n"
        "v_pk_mul_f32 v[166:167], v[166:167], v[118:119]\n"
        "v_cvt_pk_bf16_f32 v46, v152, v153\n"
        "v_cvt_pk_bf16_f32 v47, v154, v155\n"
        "v_cvt_pk_bf16_f32 v48, v156, v157\n"
        "v_cvt_pk_bf16_f32 v49, v158, v159\n"
        "v_cvt_pk_bf16_f32 v50, v160, v161\n"
        "v_cvt_pk_bf16_f32 v51, v162, v163\n"
        "v_cvt_pk_bf16_f32 v52, v164, v165\n"
        "v_cvt_pk_bf16_f32 v53, v166, v167\n"
        "ds_write_b128 v5, v[46:49]\n"
        "ds_write_b128 v6, v[50:53]\n"
        "ds_read_b128 v[62:65], v7\n"
        "ds_read_b128 v[66:69], v7 offset:1024\n"
        "s_waitcnt lgkmcnt(1)\n"
        "buffer_store_dwordx4 v[62:65], v18, s[60:63], 0 offen sc1 nt\n"
        "s_waitcnt lgkmcnt(0)\n"
        "buffer_store_dwordx4 v[66:69], v2, s[60:63], 0 offen sc1 nt\n"
        "L_fin%=:\n"
        "s_branch L_end%=\n"
        "L_grest%=:\n"
        "s_waitcnt vmcnt(0)\n"
        "v_mov_b32 v2, 0\n"
        "v_mov_b32 v3, 1\n"
        "s_mov_b64 exec, 1\n"
        "ds_add_u32 v2, v3 offset:55324\n"
        "L_gsa%=:\n"
        "ds_read_b32 v3, v2 offset:55324\n"
        "s_waitcnt lgkmcnt(0)\n"
        "v_readfirstlane_b32 s19, v3\n"
        "s_cmp_lt_u32 s19, 4\n"
        "s_cbranch_scc1 L_gsa%=\n"
        "s_mov_b64 exec, -1\n"
        "ds_read_b64_tr_b16 v[190:191], v15 offset:0\n"
        "ds_read_b64_tr_b16 v[192:193], v15 offset:1024\n"
        "ds_read_b128 v[126:129], v16 offset:0\n"
        "ds_read_b128 v[130:133], v16 offset:5440\n"
        "ds_read_b64_tr_b16 v[194:195], v15 offset:4096\n"
        "ds_read_b64_tr_b16 v[196:197], v15 offset:5120\n"
        "ds_read_b128 v[134:137], v17 offset:0\n"
        "ds_read_b128 v[138:141], v17 offset:5440\n"
        "ds_read_b64_tr_b16 v[198:199], v15 offset:8192\n"
        "ds_read_b64_tr_b16 v[200:201], v15 offset:9216\n"
        "ds_read_b128 v[142:145], v16 offset:64\n"
        "ds_read_b128 v[146:149], v16 offset:5504\n"
        "s_waitcnt lgkmcnt(8)\n"
        "v_mfma_f32_32x32x16_bf16 v[222:237], v[190:193], v[126:129], 0\n"
        "ds_read_b64_tr_b16 v[202:203], v15 offset:12288\n"
        "ds_read_b64_tr_b16 v[204:205], v15 offset:13312\n"
        "ds_read_b128 v[150:153], v17 offset:64\n"
        "ds_read_b128 v[154:157], v17 offset:5504\n"
        "v_mfma_f32_32x32x16_bf16 v[238:253], v[190:193], v[130:133], 0\n"
        "s_waitcnt lgkmcnt(8)\n"
        "v_mfma_f32_32x32x16_bf16 v[222:237], v[194:197], v[134:137], v[222:237]\n"
        "ds_read_b64_tr_b16 v[206:207], v15 offset:16384\n"
        "ds_read_b64_tr_b16 v[208:209], v15 offset:17408\n"
        "ds_read_b128 v[158:161], v16 offset:128\n"
        "ds_read_b128 v[162:165], v16 offset:5568\n"
        "v_mfma_f32_32x32x16_bf16 v[238:253], v[194:197], v[138:141], v[238:253]\n"
        "s_waitcnt lgkmcnt(8)\n"
        "v_mfma_f32_32x32x16_bf16 v[222:237], v[198:201], v[142:145], v[222:237]\n"
        "ds_read_b64_tr_b16 v[210:211], v15 offset:20480\n"
        "ds_read_b64_tr_b16 v[212:213], v15 offset:21504\n"
        "ds_read_b128 v[166:169], v17 offset:128\n"
        "ds_read_b128 v[170:173], v17 offset:5568\n"
        "v_mfma_f32_32x32x16_bf16 v[238:253], v[198:201], v[146:149], v[238:253]\n"
        "s_waitcnt lgkmcnt(8)\n"
        "v_mfma_f32_32x32x16_bf16 v[222:237], v[202:205], v[150:153], v[222:237]\n"
        "ds_read_b64_tr_b16 v[214:215], v15 offset:24576\n"
        "ds_read_b64_tr_b16 v[216:217], v15 offset:25600\n"
        "ds_read_b128 v[174:177], v16 offset:192\n"
        "ds_read_b128 v[178:181], v16 offset:5632\n"
        "s_add_u32 s18, s96, 65536\n"
        "s_mov_b32 m0, s18\n"
        "s_add_u32 s17, s96, 32768\n"
        "buffer_load_dwordx4 v8, s[52:55], s17 offen offset:0 lds\n"
        "v_mfma_f32_32x32x16_bf16 v[238:253], v[202:205], v[154:157], v[238:253]\n"
        "buffer_load_dwordx4 v8, s[52:55], s17 offen offset:1024 lds\n"
        "s_waitcnt lgkmcnt(8)\n"
        "v_mfma_f32_32x32x16_bf16 v[222:237], v[206:209], v[158:161], v[222:237]\n"
        "ds_read_b64_tr_b16 v[218:219], v15 offset:28672\n"
        "ds_read_b64_tr_b16 v[220:221], v15 offset:29696\n"
        "ds_read_b128 v[182:185], v17 offset:192\n"
        "ds_read_b128 v[186:189], v17 offset:5632\n"
        "buffer_load_dwordx4 v8, s[52:55], s17 offen offset:2048 lds\n"
        "v_mfma_f32_32x32x16_bf16 v[238:253], v[206:209], v[162:165], v[238:253]\n"
        "buffer_load_dwordx4 v8, s[52:55], s17 offen offset:3072 lds\n"
        "s_waitcnt lgkmcnt(8)\n"
        "v_mfma_f32_32x32x16_bf16 v[222:237], v[210:213], v[166:169], v[222:237]\n"
        "s_add_u32 s18, s96, 69632\n"
        "s_mov_b32 m0, s18\n"
        "s_add_u32 s17, s96, 36864\n"
        "buffer_load_dwordx4 v8, s[52:55], s17 offen offset:0 lds\n"
        "v_mfma_f32_32x32x16_bf16 v[238:253], v[210:213], v[170:173], v[238:253]\n"
        "buffer_load_dwordx4 v8, s[52:55], s17 offen offset:1024 lds\n"
        "s_waitcnt lgkmcnt(4)\n"
        "v_mfma_f32_32x32x16_bf16 v[222:237], v[214:217], v[174:177], v[222:237]\n"
        "buffer_load_dwordx4 v8, s[52:55], s17 offen offset:2048 lds\n"
        "v_mfma_f32_32x32x16_bf16 v[238:253], v[214:217], v[178:181], v[238:253]\n"
        "buffer_load_dwordx4 v8, s[52:55], s17 offen offset:3072 lds\n"
        "s_waitcnt lgkmcnt(0)\n"
        "v_mfma_f32_32x32x16_bf16 v[222:237], v[218:221], v[182:185], v[222:237]\n"
        "v_mfma_f32_32x32x16_bf16 v[238:253], v[218:221], v[186:189], v[238:253]\n"
        "s_nop 10\n"
        "v_pk_mul_f32 v[222:223], v[222:223], s[20:21]\n"
        "v_pk_mul_f32 v[224:225], v[224:225], s[20:21]\n"
        "v_pk_mul_f32 v[226:227], v[226:227], s[20:21]\n"
        "v_pk_mul_f32 v[228:229], v[228:229], s[20:21]\n"
        "v_pk_mul_f32 v[230:231], v[230:231], s[20:21]\n"
        "v_pk_mul_f32 v[232:233], v[232:233], s[20:21]\n"
        "v_pk_mul_f32 v[234:235], v[234:235], s[20:21]\n"
        "v_pk_mul_f32 v[236:237], v[236:237], s[20:21]\n"
        "v_cvt_pk_bf16_f32 v190, v222, v223\n"
        "v_cvt_pk_bf16_f32 v191, v224, v225\n"
        "v_cvt_pk_bf16_f32 v192, v226, v227\n"
        "v_cvt_pk_bf16_f32 v193, v228, v229\n"
        "v_cvt_pk_bf16_f32 v194, v230, v231\n"
        "v_cvt_pk_bf16_f32 v195, v232, v233\n"
        "v_cvt_pk_bf16_f32 v196, v234, v235\n"
        "v_cvt_pk_bf16_f32 v197, v236, v237\n"
        "v_pk_mul_f32 v[238:239], v[238:239], s[20:21]\n"
        "v_pk_mul_f32 v[240:241], v[240:241], s[20:21]\n"
        "v_pk_mul_f32 v[242:243], v[242:243], s[20:21]\n"
        "v_pk_mul_f32 v[244:245], v[244:245], s[20:21]\n"
        "v_pk_mul_f32 v[246:247], v[246:247], s[20:21]\n"
        "v_pk_mul_f32 v[248:249], v[248:249], s[20:21]\n"
        "v_pk_mul_f32 v[250:251], v[250:251], s[20:21]\n"
        "v_pk_mul_f32 v[252:253], v[252:253], s[20:21]\n"
        "v_cvt_pk_bf16_f32 v198, v238, v239\n"
        "v_cvt_pk_bf16_f32 v199, v240, v241\n"
        "v_cvt_pk_bf16_f32 v200, v242, v243\n"
        "v_cvt_pk_bf16_f32 v201, v244, v245\n"
        "v_cvt_pk_bf16_f32 v202, v246, v247\n"
        "v_cvt_pk_bf16_f32 v203, v248, v249\n"
        "v_cvt_pk_bf16_f32 v204, v250, v251\n"
        "v_cvt_pk_bf16_f32 v205, v252, v253\n"
        "s_mov_b64 exec, s[26:27]\n"
        "ds_write_b128 v108, v[190:193]\n"
        "ds_write_b128 v109, v[194:197]\n"
        "ds_write_b128 v110, v[198:201] offset:1280\n"
        "ds_write_b128 v111, v[202:205] offset:1280\n"
        "s_mov_b64 exec, -1\n"
        "ds_read_b128 v[206:209], v103 offset:0\n"
        "ds_read_b128 v[210:213], v103 offset:1024\n"
        "ds_read_b128 v[214:217], v103 offset:2048\n"
        "v_add_u32 v2, 0x100000, v18\n"
        "v_add_u32 v3, 0x200000, v18\n"
        "s_waitcnt lgkmcnt(2)\n"
        "buffer_store_dwordx4 v[206:209], v18, s[68:71], 0 offen sc1\n"
        "s_waitcnt lgkmcnt(1)\n"
        "buffer_store_dwordx4 v[210:213], v2, s[68:71], 0 offen sc1\n"
        "s_mov_b32 exec_hi, 0\n"
        "s_waitcnt lgkmcnt(0)\n"
        "buffer_store_dwordx4 v[214:217], v3, s[68:71], 0 offen sc1\n"
        "s_mov_b32 exec_hi, -1\n"
        "v_subrev_u32 v7, 65536, v15\n"
        "s_waitcnt vmcnt(3)\n"
        "v_readfirstlane_b32 s10, v124\n"
        "s_add_u32 s10, s10, 0x400001\n"
        "v_mov_b64 v[26:27], 0\n"
        "v_mov_b64 v[28:29], 0\n"
        "v_lshlrev_b32 v2, 3, v10\n"
        "v_sub_u32 v2, v9, v2\n"
        "v_mfma_f32_32x32x16_bf16 v[46:61], v[26:29], v[26:29], 0\n"
        "v_lshlrev_b32 v2, 4, v2\n"
        "v_mov_b32 v3, 0x3f80\n"
        "v_mov_b32 v5, 0\n"
        "v_cmp_gt_u32 vcc, 64, v2\n"
        "s_nop 1\n"
        "v_cndmask_b32 v4, 0, v3, vcc\n"
        "v_lshlrev_b64 v[22:23], v2, v[4:5]\n"
        "v_subrev_u32 v6, 64, v2\n"
        "v_cmp_gt_u32 vcc, 64, v6\n"
        "v_mfma_f32_32x32x16_bf16 v[62:77], v[26:29], v[26:29], 0\n"
        "s_nop 0\n"
        "v_cndmask_b32 v4, 0, v3, vcc\n"
        "v_lshlrev_b64 v[24:25], v6, v[4:5]\n"
        "v_subrev_u32 v6, 256, v2\n"
        "v_cmp_gt_u32 vcc, 64, v6\n"
        "v_mfma_f32_32x32x16_bf16 v[78:93], v[26:29], v[26:29], 0\n"
        "s_nop 0\n"
        "v_cndmask_b32 v4, 0, v3, vcc\n"
        "v_lshlrev_b64 v[26:27], v6, v[4:5]\n"
        "v_subrev_u32 v6, 320, v2\n"
        "v_cmp_gt_u32 vcc, 64, v6\n"
        "s_nop 1\n"
        "v_cndmask_b32 v4, 0, v3, vcc\n"
        "v_lshlrev_b64 v[28:29], v6, v[4:5]\n"
        "v_bfe_u32 v2, v9, 1, 2\n"
        "v_xor_b32 v2, v2, v10\n"
        "v_lshlrev_b32 v2, 4, v2\n"
        "v_lshl_or_b32 v2, v9, 7, v2\n"
        "v_add_u32 v21, s12, v2\n"
        "v_xor_b32 v30, 32, v21\n"
        "v_lshlrev_b32 v2, 4, v10\n"
        "v_lshl_or_b32 v31, v9, 5, v2\n"
        "v_lshl_add_u32 v2, v10, 2, v11\n"
        "v_add_u32 v2, v2, v13\n"
        "v_lshlrev_b32 v3, 3, v12\n"
        "v_lshl_or_b32 v32, v2, 5, v3\n"
        "v_lshrrev_b32 v2, 3, v1\n"
        "s_movk_i32 s19, 28672\n"
        "v_lshl_add_u32 v33, v2, 2, s19\n"
        "v_bfe_u32 v2, v1, 4, 2\n"
        "v_and_b32 v4, 7, v1\n"
        "v_xor_b32 v2, v2, v4\n"
        "s_lshl_b32 s19, s15, 7\n"
        "v_lshl_add_u32 v45, v2, 4, s19\n"
        "v_add_u32 v39, 3072, v45\n"
        "v_add_u32 v41, 2048, v45\n"
        "v_add_u32 v43, 1024, v45\n"
        "s_sub_u32 s68, s68, 3072\n"
        "s_subb_u32 s69, s69, 0\n"
        "s_or_b32 s69, s69, 0x1000000\n"
        "s_lshr_b32 s70, s70, 8\n"
        "v_lshl_or_b32 v3, v13, 5, v3\n"
        "v_or_b32 v3, s19, v3\n"
        "v_lshlrev_b32 v2, 6, v11\n"
        "v_xor_b32 v3, v3, v2\n"
        "v_add_u32 v34, 43776, v3\n"
        "v_cmp_eq_u32 vcc, 0, v10\n"
        "v_and_b32 v2, 1, v11\n"
        "v_or_b32 v2, 8, v2\n"
        "v_cndmask_b32 v2, v2, v11, vcc\n"
        "v_lshl_add_u32 v35, v2, 10, v34\n"
        "v_lshl_add_u32 v2, v11, 2, 16\n"
        "v_add_u32 v3, 40, v11\n"
        "v_cndmask_b32 v2, v3, v2, vcc\n"
        "v_lshl_add_u32 v36, v2, 8, v34\n"
        "v_mov_b32 v2, 0x100\n"
        "v_cndmask_b32 v34, 0, v2, vcc\n"
        "s_setprio 3\n"
        "s_waitcnt vmcnt(0)\n"
        "v_mov_b32 v2, 0\n"
        "v_mov_b32 v3, 1\n"
        "s_mov_b64 exec, 1\n"
        "ds_add_rtn_u32 v2, v2, v3 offset:55320\n"
        "s_mov_b64 exec, 0x1\n"
        "s_lshl_b32 s19, s9, 7\n"
        "s_add_u32 s19, s19, 1024\n"
        "v_mul_u32_u24 v3, 32896, v1\n"
        "v_mov_b32 v4, s10\n"
        "s_waitcnt lgkmcnt(0)\n"
        "v_readfirstlane_b32 s18, v2\n"
        "s_cmp_lg_u32 s18, 3\n"
        "s_cbranch_scc1 L_gsig%=\n"
        "buffer_store_dword v4, v3, s[64:67], s19 offen sc1\n"
        "L_gsig%=:\n"
        "s_mov_b64 exec, -1\n"
        "s_setprio 1\n"
        "v_mov_b32 v2, 0\n"
        "v_mov_b32 v3, 1\n"
        "s_mov_b64 exec, 1\n"
        "ds_add_u32 v2, v3 offset:55336\n"
        "L_gsb%=:\n"
        "ds_read_b32 v3, v2 offset:55336\n"
        "s_waitcnt lgkmcnt(0)\n"
        "v_readfirstlane_b32 s19, v3\n"
        "s_cmp_lt_u32 s19, 4\n"
        "s_cbranch_scc1 L_gsb%=\n"
        "s_mov_b64 exec, -1\n"
        "ds_read_b64_tr_b16 v[190:191], v7 offset:0\n"
        "ds_read_b64_tr_b16 v[192:193], v7 offset:1024\n"
        "ds_read_b64_tr_b16 v[194:195], v7 offset:4096\n"
        "ds_read_b64_tr_b16 v[196:197], v7 offset:5120\n"
        "ds_read_b64_tr_b16 v[198:199], v7 offset:8192\n"
        "ds_read_b64_tr_b16 v[200:201], v7 offset:9216\n"
        "ds_read_b64_tr_b16 v[202:203], v7 offset:12288\n"
        "ds_read_b64_tr_b16 v[204:205], v7 offset:13312\n"
        "s_waitcnt lgkmcnt(6)\n"
        "v_mfma_f32_32x32x16_bf16 v[222:237], v[190:193], v[126:129], 0\n"
        "ds_read_b64_tr_b16 v[206:207], v7 offset:16384\n"
        "ds_read_b64_tr_b16 v[208:209], v7 offset:17408\n"
        "v_lshrrev_b32 v2, 2, v9\n"
        "v_lshlrev_b32 v4, 5, v10\n"
        "v_lshl_add_u32 v4, v9, 8, v4\n"
        "v_mfma_f32_32x32x16_bf16 v[238:253], v[190:193], v[130:133], 0\n"
        "v_xor_b32 v3, s13, v2\n"
        "v_and_b32 v3, 3, v3\n"
        "v_lshl_add_u32 v112, v3, 6, v4\n"
        "s_waitcnt lgkmcnt(6)\n"
        "v_mfma_f32_32x32x16_bf16 v[222:237], v[194:197], v[134:137], v[222:237]\n"
        "ds_read_b64_tr_b16 v[210:211], v7 offset:20480\n"
        "ds_read_b64_tr_b16 v[212:213], v7 offset:21504\n"
        "v_add_u32 v2, 1, v2\n"
        "v_xor_b32 v3, s13, v2\n"
        "v_and_b32 v3, 3, v3\n"
        "v_mfma_f32_32x32x16_bf16 v[238:253], v[194:197], v[138:141], v[238:253]\n"
        "v_lshl_add_u32 v113, v3, 6, v4\n"
        "v_lshlrev_b32 v94, 16, v104\n"
        "v_and_b32 v95, 0xffff0000, v104\n"
        "s_waitcnt lgkmcnt(6)\n"
        "v_mfma_f32_32x32x16_bf16 v[222:237], v[198:201], v[142:145], v[222:237]\n"
        "ds_read_b64_tr_b16 v[214:215], v7 offset:24576\n"
        "ds_read_b64_tr_b16 v[216:217], v7 offset:25600\n"
        "v_lshlrev_b32 v96, 16, v105\n"
        "v_and_b32 v97, 0xffff0000, v105\n"
        "v_lshlrev_b32 v98, 16, v106\n"
        "v_mfma_f32_32x32x16_bf16 v[238:253], v[198:201], v[146:149], v[238:253]\n"
        "v_and_b32 v99, 0xffff0000, v106\n"
        "v_lshlrev_b32 v100, 16, v107\n"
        "v_and_b32 v101, 0xffff0000, v107\n"
        "s_waitcnt lgkmcnt(6)\n"
        "v_mfma_f32_32x32x16_bf16 v[222:237], v[202:205], v[150:153], v[222:237]\n"
        "ds_read_b64_tr_b16 v[218:219], v7 offset:28672\n"
        "ds_read_b64_tr_b16 v[220:221], v7 offset:29696\n"
        "v_mul_f32 v94, s20, v94\n"
        "v_mul_f32 v95, s20, v95\n"
        "v_mul_f32 v96, s20, v96\n"
        "v_mfma_f32_32x32x16_bf16 v[238:253], v[202:205], v[154:157], v[238:253]\n"
        "v_mul_f32 v97, s20, v97\n"
        "v_mul_f32 v98, s20, v98\n"
        "v_mul_f32 v99, s20, v99\n"
        "s_waitcnt lgkmcnt(6)\n"
        "v_mfma_f32_32x32x16_bf16 v[222:237], v[206:209], v[158:161], v[222:237]\n"
        "v_mul_f32 v100, s20, v100\n"
        "v_mul_f32 v101, s20, v101\n"
        "v_cvt_pk_bf16_f32 v104, v94, v95\n"
        "v_mfma_f32_32x32x16_bf16 v[238:253], v[206:209], v[162:165], v[238:253]\n"
        "v_cvt_pk_bf16_f32 v105, v96, v97\n"
        "v_cvt_pk_bf16_f32 v106, v98, v99\n"
        "v_cvt_pk_bf16_f32 v107, v100, v101\n"
        "s_waitcnt lgkmcnt(4)\n"
        "v_mfma_f32_32x32x16_bf16 v[222:237], v[210:213], v[166:169], v[222:237]\n"
        "s_lshl_b32 s16, s8, 2\n"
        "s_lshl_b32 s19, s8, 8\n"
        "v_xor_b32 v2, s16, v1\n"
        "v_mfma_f32_32x32x16_bf16 v[238:253], v[210:213], v[170:173], v[238:253]\n"
        "v_lshl_add_u32 v2, v2, 4, s19\n"
        "s_waitcnt lgkmcnt(2)\n"
        "v_mfma_f32_32x32x16_bf16 v[222:237], v[214:217], v[174:177], v[222:237]\n"
        "v_mfma_f32_32x32x16_bf16 v[238:253], v[214:217], v[178:181], v[238:253]\n"
        "s_waitcnt lgkmcnt(0)\n"
        "v_mfma_f32_32x32x16_bf16 v[222:237], v[218:221], v[182:185], v[222:237]\n"
        "v_mfma_f32_32x32x16_bf16 v[238:253], v[218:221], v[186:189], v[238:253]\n"
        "s_mov_b64 exec, 0xffff\n"
        "ds_write_b128 v2, v[104:107] offset:54016\n"
        "s_mov_b64 exec, -1\n"
        "s_nop 7\n"
        "v_pk_mul_f32 v[222:223], v[222:223], s[20:21]\n"
        "v_pk_mul_f32 v[224:225], v[224:225], s[20:21]\n"
        "v_pk_mul_f32 v[226:227], v[226:227], s[20:21]\n"
        "v_pk_mul_f32 v[228:229], v[228:229], s[20:21]\n"
        "v_pk_mul_f32 v[230:231], v[230:231], s[20:21]\n"
        "v_pk_mul_f32 v[232:233], v[232:233], s[20:21]\n"
        "v_pk_mul_f32 v[234:235], v[234:235], s[20:21]\n"
        "v_pk_mul_f32 v[236:237], v[236:237], s[20:21]\n"
        "v_cvt_pk_bf16_f32 v190, v222, v223\n"
        "v_cvt_pk_bf16_f32 v191, v224, v225\n"
        "v_cvt_pk_bf16_f32 v192, v226, v227\n"
        "v_cvt_pk_bf16_f32 v193, v228, v229\n"
        "v_cvt_pk_bf16_f32 v194, v230, v231\n"
        "v_cvt_pk_bf16_f32 v195, v232, v233\n"
        "v_cvt_pk_bf16_f32 v196, v234, v235\n"
        "v_cvt_pk_bf16_f32 v197, v236, v237\n"
        "v_pk_mul_f32 v[238:239], v[238:239], s[20:21]\n"
        "v_pk_mul_f32 v[240:241], v[240:241], s[20:21]\n"
        "v_pk_mul_f32 v[242:243], v[242:243], s[20:21]\n"
        "v_pk_mul_f32 v[244:245], v[244:245], s[20:21]\n"
        "v_pk_mul_f32 v[246:247], v[246:247], s[20:21]\n"
        "v_pk_mul_f32 v[248:249], v[248:249], s[20:21]\n"
        "v_pk_mul_f32 v[250:251], v[250:251], s[20:21]\n"
        "v_pk_mul_f32 v[252:253], v[252:253], s[20:21]\n"
        "v_cvt_pk_bf16_f32 v198, v238, v239\n"
        "v_cvt_pk_bf16_f32 v199, v240, v241\n"
        "v_cvt_pk_bf16_f32 v200, v242, v243\n"
        "v_cvt_pk_bf16_f32 v201, v244, v245\n"
        "v_cvt_pk_bf16_f32 v202, v246, v247\n"
        "v_cvt_pk_bf16_f32 v203, v248, v249\n"
        "v_cvt_pk_bf16_f32 v204, v250, v251\n"
        "v_cvt_pk_bf16_f32 v205, v252, v253\n"
        "s_mov_b64 exec, s[26:27]\n"
        "ds_write_b128 v112, v[190:193] offset:43776\n"
        "ds_write_b128 v112, v[194:197] offset:43792\n"
        "ds_write_b128 v113, v[198:201] offset:48896\n"
        "ds_write_b128 v113, v[202:205] offset:48912\n"
        "s_mov_b64 exec, -1\n"
        "s_add_u32 s18, s96, 131072\n"
        "s_mov_b32 m0, s18\n"
        "s_add_u32 s17, s96, 0\n"
        "buffer_load_dwordx4 v8, s[56:59], s17 offen offset:0 lds\n"
        "buffer_load_dwordx4 v8, s[56:59], s17 offen offset:1024 lds\n"
        "buffer_load_dwordx4 v8, s[56:59], s17 offen offset:2048 lds\n"
        "buffer_load_dwordx4 v8, s[56:59], s17 offen offset:3072 lds\n"
        "s_add_u32 s18, s96, 135168\n"
        "s_mov_b32 m0, s18\n"
        "s_add_u32 s17, s96, 4096\n"
        "buffer_load_dwordx4 v8, s[56:59], s17 offen offset:0 lds\n"
        "buffer_load_dwordx4 v8, s[56:59], s17 offen offset:1024 lds\n"
        "buffer_load_dwordx4 v8, s[56:59], s17 offen offset:2048 lds\n"
        "buffer_load_dwordx4 v8, s[56:59], s17 offen offset:3072 lds\n"
        "s_and_b32 s19, %2, 0\n"
        "s_mul_i32 s19, s19, 32896\n"
        "s_lshl_b32 s18, s8, 13\n"
        "s_add_u32 s19, s19, s18\n"
        "s_add_u32 s19, s19, 1024\n"
        "v_lshl_add_u32 v6, v1, 7, s19\n"
        "s_mov_b32 s22, 0\n"
        "s_sleep 4\n"
        "LF_chk%=:\n"
        "buffer_load_dword v7, v6, s[64:67], 0 offen sc1\n"
        "s_waitcnt vmcnt(0)\n"
        "LF_tst%=:\n"
        "v_cmpx_ne_u32 vcc, s10, v7\n"
        "s_cbranch_execz LF_ok%=\n"
        "s_add_u32 s22, s22, 1\n"
        "s_cmp_gt_u32 s22, 8192\n"
        "s_cbranch_scc1 L_tmof%=\n"
        "s_sleep 6\n"
        "s_branch LF_chk%=\n"
        "L_tmof%=:\n"
        "v_mov_b32 v2, 0\n"
        "v_mov_b32 v3, 1\n"
        "ds_write_b32 v2, v3 offset:55312\n"
        "ds_write_b32 v2, v3 offset:55316\n"
        "LF_ok%=:\n"
        "s_mov_b64 exec, -1\n"
        "s_waitcnt vmcnt(0)\n"
        "s_branch L_join%=\n"
        "L_end%=:\n"
        "s_waitcnt vmcnt(0) lgkmcnt(0)\n"
        : "=&s"(slow)
        : "s"(__builtin_amdgcn_kernarg_segment_ptr()), "s"(blockIdx.x), "{v0}"(threadIdx.x)
        : "v1", "v2", "v3", "v4", "v5", "v6", "v7", "v8", "v9", "v10", "v11", "v12", "v13", "v14", "v15", "v16", "v17", "v18", "v19", "v20", "v21", "v22", "v23", "v24", "v25", "v26", "v27", "v28", "v29", "v30", "v31", "v32", "v33", "v34", "v35", "v36", "v37", "v38", "v39", "v40", "v41", "v42", "v43", "v44", "v45", "v46", "v47", "v48", "v49", "v50", "v51", "v52", "v53", "v54", "v55", "v56", "v57", "v58", "v59", "v60", "v61", "v62", "v63", "v64", "v65", "v66", "v67", "v68", "v69", "v70", "v71", "v72", "v73", "v74", "v75", "v76", "v77", "v78", "v79", "v80", "v81", "v82", "v83", "v84", "v85", "v86", "v87", "v88", "v89", "v90", "v91", "v92", "v93", "v94", "v95", "v96", "v97", "v98", "v99", "v100", "v101", "v102", "v103", "v104", "v105", "v106", "v107", "v108", "v109", "v110", "v111", "v112", "v113", "v114", "v115", "v116", "v117", "v118", "v119", "v120", "v121", "v122", "v123", "v124", "v125", "v126", "v127", "v128", "v129", "v130", "v131", "v132", "v133", "v134", "v135", "v136", "v137", "v138", "v139", "v140", "v141", "v142", "v143", "v144", "v145", "v146", "v147", "v148", "v149", "v150", "v151", "v152", "v153", "v154", "v155", "v156", "v157", "v158", "v159", "v160", "v161", "v162", "v163", "v164", "v165", "v166", "v167", "v168", "v169", "v170", "v171", "v172", "v173", "v174", "v175", "v176", "v177", "v178", "v179", "v180", "v181", "v182", "v183", "v184", "v185", "v186", "v187", "v188", "v189", "v190", "v191", "v192", "v193", "v194", "v195", "v196", "v197", "v198", "v199", "v200", "v201", "v202", "v203", "v204", "v205", "v206", "v207", "v208", "v209", "v210", "v211", "v212", "v213", "v214", "v215", "v216", "v217", "v218", "v219", "v220", "v221", "v222", "v223", "v224", "v225", "v226", "v227", "v228", "v229", "v230", "v231", "v232", "v233", "v234", "v235", "v236", "v237", "v238", "v239", "v240", "v241", "v242", "v243", "v244", "v245", "v246", "v247", "v248", "v249", "v250", "v251", "v252", "v253", "s8", "s9", "s10", "s11", "s12", "s13", "s14", "s15", "s16", "s17", "s18", "s19", "s20", "s21", "s22", "s23", "s24", "s25", "s26", "s27", "s28", "s29", "s30", "s31", "s33", "s34", "s35", "s36", "s37", "s38", "s39", "s40", "s41", "s42", "s43", "s44", "s45", "s46", "s47", "s48", "s49", "s50", "s51", "s52", "s53", "s54", "s55", "s56", "s57", "s58", "s59", "s60", "s61", "s62", "s63", "s64", "s65", "s66", "s67", "s68", "s69", "s70", "s71", "s72", "s73", "s74", "s75", "s76", "s77", "s78", "s79", "s80", "s81", "s82", "s83", "s84", "s85", "s86", "s87", "s88", "s89", "s90", "s91", "s92", "s93", "s94", "s95", "s96", "s97", "s98", "s99", "vcc", "scc", "memory");
    if (slow) {
        const int b = blockIdx.x;
        if (threadIdx.x == 0) smem[163839] = 0;
        slow_rows(smem, x, ea, src, dst, wm, wu, out, nx >> 8, E, (b & 0xc0) | ((b & 7) << 3) | ((b >> 3) & 7));
    }
#endif
}

__global__ void __launch_bounds__(512)
mpnn_generic(const uint16_t* x, const uint16_t* ea, const int* src, const int* dst, const uint16_t* wm, const uint16_t* wu, uint16_t* out, int N, int E) {
    __shared__ __attribute__((aligned(16))) char smem[22544];
    slow_rows(smem, x, ea, src, dst, wm, wu, out, N, E, (blockIdx.x >> 8) * 10240 + (blockIdx.x & 255));
}

extern "C" void kernel_launch(void* const* d_in, const int* in_sizes, int n_in, void* d_out, int out_size, void* d_ws, size_t ws_size,
                              hipStream_t stream) {
    const u32 N = in_sizes[0] / 128, E = in_sizes[2], CH = E / 64, F = 0x00020000u;
    const uint16_t *x = (const uint16_t*)d_in[0], *ea = (const uint16_t*)d_in[1], *wm = (const uint16_t*)d_in[4], *wu = (const uint16_t*)d_in[5];
    const int *src = (const int*)d_in[2], *dst = (const int*)d_in[3];
    if (N > 10240 || E % 256 != 0 || CH <= 2048 || CH > 2560 || ws_size < 0u + 6631424u + (size_t)N * 256) {
        mpnn_generic<<<(N + 10239) / 10240 * 256, 512, 0, stream>>>(x, ea, src, dst, wm, wu, (uint16_t*)d_out, (int)N, (int)E);
        return;
    }
    char* ws = (char*)d_ws + 0u;
    mpnn_kernel<<<256, 512, 0, stream>>>(x, N * 256, F, ea, E * 8, F, src, E * 4, F, dst, E * 4, F, wm, 260 * 256, F, wu, 256 * 256, F,
                                         (uint16_t*)d_out, N * 256, F, ws, 6627328u, F, ws + 6631424, N * 256, F, (int)E, (int)CH);
}
```
